```python
import torch

def _softmax(x, axis=-1):
    return torch.softmax(x, dim=axis)
def _gelu(x):
    return torch.nn.functional.gelu(x, approximate="tanh")
def _silu(x):
    return torch.nn.functional.silu(x)
def _sigmoid(x):
    return torch.sigmoid(x)
def _layernorm(x, axis=-1, eps=1e-5):
    m = x.mean(dim=axis, keepdim=True)
    v = x.var(dim=axis, keepdim=True, unbiased=False)
    return (x - m) / torch.sqrt(v + eps)
def _scatter_add(tgt, idx, src, axis=0):
    return tgt.clone().index_add_(axis, idx, src)
def _segment_sum(src, seg, n):
    out = torch.zeros((n,) + src.shape[1:], dtype=src.dtype)
    return out.index_add_(0, seg, src)
def _pad(x, pw):
    flat = tuple(v for pair in reversed(pw) for v in pair)
    return torch.nn.functional.pad(x, flat)

kernel_name = 'standalone__glu_gated_geglu__sampling'

def setup_inputs():

    _w = (lambda a: (lambda t: t.to(torch.bfloat16) if t.is_floating_point() else t)(torch.as_tensor(a)))
    B, S, Dm, Df = 8, 4, 1024, 3584
    return {
        "x": _w((lambda *s: torch.randn(*s, dtype=torch.bfloat16) * 0.05)(B, S, Dm)),
        "w_gate": _w((lambda *s: torch.randn(*s, dtype=torch.bfloat16) * 0.05)(Dm, Df) * (Dm ** -0.5)),
        "w_up": _w((lambda *s: torch.randn(*s, dtype=torch.bfloat16) * 0.05)(Dm, Df) * (Dm ** -0.5)),
        "w_down": _w((lambda *s: torch.randn(*s, dtype=torch.bfloat16) * 0.05)(Df, Dm) * (Df ** -0.5)),
    }

def reference(x, w_gate, w_up, w_down):
    g = torch.einsum("bsd,df->bsf", x, w_gate)
    u = torch.einsum("bsd,df->bsf", x, w_up)
    h = _gelu(g) * u
    return torch.einsum("bsf,fd->bsd", h, w_down)
```

```cpp
#include <hip/hip_runtime.h>
#include <hip/hip_bf16.h>
#include <stdint.h>

constexpr int M = 32, DM = 1024, DF = 3584;

__device__ __forceinline__ float bf2f(unsigned short b) { return __builtin_bit_cast(float, (unsigned)b << 16); }
__device__ __forceinline__ unsigned short f2bf(float f) { __bf16 b = (__bf16)f; return __builtin_bit_cast(unsigned short, b); }

__global__ void k1(const unsigned short* x, const unsigned short* wg, const unsigned short* wu, unsigned short* h) {
    int f = blockIdx.x * 64 + threadIdx.x;
    int t = blockIdx.y;
    float g = 0, u = 0;
    for (int k = 0; k < DM; ++k) {
        float xv = bf2f(x[t * DM + k]);
        g += xv * bf2f(wg[k * DF + f]);
        u += xv * bf2f(wu[k * DF + f]);
    }
    g = bf2f(f2bf(g)); u = bf2f(f2bf(u));
    float y = 0.7978845608028654f * (g + 0.044715f * g * g * g);
    float ge = g / (1.0f + __expf(-2.0f * y));
    ge = bf2f(f2bf(ge));
    h[t * DF + f] = f2bf(ge * u);
}
__global__ void k2(const unsigned short* h, const unsigned short* wd, unsigned short* out) {
    int n = blockIdx.x * 64 + threadIdx.x;
    int t = blockIdx.y;
    float a = 0;
    for (int k = 0; k < DF; ++k) a += bf2f(h[t * DF + k]) * bf2f(wd[k * DM + n]);
    out[t * DM + n] = f2bf(a);
}

extern "C" void kernel_launch(void* const* d_in, const int* in_sizes, int n_in,
                              void* d_out, int out_size, void* d_ws, size_t ws_size,
                              hipStream_t stream) {
    const unsigned short* x = (const unsigned short*)d_in[0];
    const unsigned short* wg = (const unsigned short*)d_in[1];
    const unsigned short* wu = (const unsigned short*)d_in[2];
    const unsigned short* wd = (const unsigned short*)d_in[3];
    unsigned short* h = (unsigned short*)d_ws;
    k1<<<dim3(DF / 64, M), 64, 0, stream>>>(x, wg, wu, h);
    k2<<<dim3(DM / 64, M), 64, 0, stream>>>(h, wd, (unsigned short*)d_out);
}
```

```cpp
#include <hip/hip_runtime.h>
#include <stdint.h>

#define WAUX 0
#define DAUX 0
#define OAUX 0
#define PREWC 0
#define CDELAY 8

#define LAS __attribute__((address_space(3)))
typedef unsigned v4u __attribute__((ext_vector_type(4)));
typedef unsigned v2u __attribute__((ext_vector_type(2)));
typedef float f32x4 __attribute__((ext_vector_type(4)));
typedef float f32x2 __attribute__((ext_vector_type(2)));
typedef short s16x8 __attribute__((ext_vector_type(8)));
typedef short s16x4 __attribute__((ext_vector_type(4)));
typedef __bf16 bf16x8 __attribute__((ext_vector_type(8)));
typedef __bf16 bf16x2 __attribute__((ext_vector_type(2)));
typedef unsigned long long u64;

constexpr int DM = 1024, DF = 3584;
constexpr int SC1 = 16;
constexpr int SC1V = 16 | (1 << 31);
constexpr int WS_EPOCH = 0, WS_FLAG = 4096, WS_H = 65536, WS_PART = 1 << 20, WS_BYTES = WS_PART + 16 * 16 * 16 * 512;
constexpr int L_RED = 0, RED_PAR = 1024 + 128, RED_TT = 2 * RED_PAR, RED_W = 2 * RED_TT;
constexpr int L_WD = 8 * RED_W;
constexpr int L_H = L_WD + 28 * 1024;
constexpr int L_TAG = L_H + 14 * 1024;
constexpr int L_BYTES = 84 * 1024;
static_assert(L_TAG + 16 <= L_BYTES, "lds");
constexpr unsigned SPIN_CAP = 1u << 16;

#define STAMP(i) do {} while (0)

__device__ __forceinline__ f32x4 mfma(v4u a, v4u b, f32x4 c) {
    return __builtin_amdgcn_mfma_f32_16x16x32_bf16(__builtin_bit_cast(bf16x8, a), __builtin_bit_cast(bf16x8, b), c, 0, 0, 0);
}
__device__ __forceinline__ unsigned pack_bf16(float lo, float hi) {
    bf16x2 r; r.x = (__bf16)lo; r.y = (__bf16)hi; return __builtin_bit_cast(unsigned, r);
}
__device__ __forceinline__ float lo_f(unsigned d) { return __builtin_bit_cast(float, d << 16); }
__device__ __forceinline__ float hi_f(unsigned d) { return __builtin_bit_cast(float, d & 0xffff0000u); }
__device__ __forceinline__ float gelu_tanh(float g) {
    const float y2 = g * (2.0f * 0.7978845608028654f * 1.4426950408889634f) * (1.0f + 0.044715f * g * g);
    return g * __builtin_amdgcn_rcpf(1.0f + __builtin_amdgcn_exp2f(-y2));
}
#define BARRIER() do { asm volatile("s_waitcnt lgkmcnt(0)" ::: "memory"); __builtin_amdgcn_s_barrier(); asm volatile("" ::: "memory"); } while (0)

__global__ void __launch_bounds__(576, 1) geglu_kernel(const void* __restrict__ x, const void* __restrict__ wg, const void* __restrict__ wu,
                                                      const void* __restrict__ wd, void* __restrict__ out, void* ws) {
    extern __shared__ __attribute__((aligned(16))) unsigned char smem[];
    LAS unsigned char* L = (LAS unsigned char*)smem;
    STAMP(0);
    asm volatile("" :: "s"(x), "s"(wg), "s"(wu), "s"(wd), "s"(out), "s"(ws));
    const int tid = threadIdx.x, lane = tid & 63, w = __builtin_amdgcn_readfirstlane(tid >> 6);
    const int xl = blockIdx.x & 7, li = blockIdx.x >> 3;
    const bool prod = li < 28;
    const int mat = li & 1, tile = 14 * xl + (li >> 1);
    const int s = 2 * xl + (li >> 4), cg = li & 15;
    const __amdgpu_buffer_rsrc_t rws = __builtin_amdgcn_make_buffer_rsrc(ws, 0, WS_BYTES, 0x00020000);

    if (w == 8) {
        if (prod) {
            const int ho = WS_H + (mat * 112 + tile) * 2048 + lane * 16;
            const v4u ones = {0xffffffffu, 0xffffffffu, 0xffffffffu, 0xffffffffu};
            __builtin_amdgcn_raw_buffer_store_b128(ones, rws, ho, 0, SC1);
            __builtin_amdgcn_raw_buffer_store_b128(ones, rws, ho + 1024, 0, SC1);
        }
        v2u ep = __builtin_amdgcn_raw_buffer_load_b64(rws, WS_EPOCH, 0, SC1);
        asm volatile("s_waitcnt vmcnt(0)" : "+v"(ep) :: "memory");
        const unsigned elo = __builtin_amdgcn_readfirstlane(ep.x), ehi = __builtin_amdgcn_readfirstlane(ep.y);
        const u64 tag = (((u64)ehi << 32) | elo) + 1u;
        const v2u tagv = {(unsigned)tag, (unsigned)(tag >> 32)};
        if (lane == 0) __builtin_amdgcn_raw_buffer_store_b64(tagv, rws, WS_FLAG + (xl * 32 + li) * 128, 0, SC1);
        STAMP(1);
        {
            const int pl = lane < 14 ? lane : 0;
            const int fo = WS_FLAG + (xl * 32 + 14 * (li >> 4) + pl) * 128;
#pragma unroll 1
            for (unsigned spin = 0; spin < SPIN_CAP; ++spin) {
                asm volatile("" ::: "memory");
                const v2u f = __builtin_amdgcn_raw_buffer_load_b64(rws, fo, 0, SC1V);
                if (__all(f.x == tagv.x && f.y == tagv.y)) break;
                __builtin_amdgcn_s_sleep(2);
            }
        }
        STAMP(2);
        if (lane == 0) *(LAS v2u*)(L + L_TAG) = tagv;
        {
            const __amdgpu_buffer_rsrc_t rd = __builtin_amdgcn_make_buffer_rsrc((void*)wd, 0, DM * DF * 2, 0x00020000);
            const int rho = lane >> 3, cpos = lane & 7;
            const int srow = ((rho & 3) << 1) | (rho >> 2);
            const int f0 = (rho >> 1) & 1;
            const int vb = (s * 224 + srow) * (DM * 2) + cg * 128;
            const int v0 = vb + ((cpos ^ (f0 << 1)) << 4), v1 = vb + ((cpos ^ ((f0 | 2) << 1)) << 4);
#pragma unroll
            for (int g = 0; g < 28; ++g)
                __builtin_amdgcn_raw_ptr_buffer_load_lds(rd, (LAS void*)(L + L_WD + g * 1024), 16, (g & 1) ? v1 : v0, g * 8 * (DM * 2), 0, DAUX);
        }
        BARRIER();
        asm volatile("s_waitcnt vmcnt(0)" ::: "memory");
        STAMP(3);
        BARRIER();
        BARRIER();
        STAMP(4);
        for (int i = 0; i < CDELAY; ++i) __builtin_amdgcn_s_sleep(1);
        const int io = WS_PART + (cg * 16 + s) * 8192 + lane * 16;
        const unsigned t8 = tagv.x & 0xffu;
        v4u p[8];
#pragma unroll 1
        for (unsigned spin = 0; spin < SPIN_CAP; ++spin) {
            asm volatile("" ::: "memory");
#pragma unroll
            for (int i = 0; i < 8; ++i) p[i] = __builtin_amdgcn_raw_buffer_load_b128(rws, io, i * 1024, SC1V);
            unsigned bad = 0;
#pragma unroll
            for (int i = 0; i < 8; ++i) bad |= (p[i].x ^ t8) | (p[i].y ^ t8) | (p[i].z ^ t8) | (p[i].w ^ t8);
            if (__all((bad & 0xffu) == 0)) break;
        }
        STAMP(5);
        f32x4 acc = {0.f, 0.f, 0.f, 0.f};
#pragma unroll
        for (int i = 0; i < 8; ++i) acc += __builtin_bit_cast(f32x4, p[i] & 0xffffff00u);
        f32x4 o;
#pragma unroll
        for (int j = 0; j < 4; ++j) o[j] = acc[j] + __shfl_xor(acc[j], 32);
        if (lane < 32) {
            const __amdgpu_buffer_rsrc_t ro = __builtin_amdgcn_make_buffer_rsrc(out, 0, 32 * DM * 2, 0x00020000);
            const v2u ov = {pack_bf16(o[0], o[1]), pack_bf16(o[2], o[3])};
            __builtin_amdgcn_raw_buffer_store_b64(ov, ro, (2 * s + (lane >> 4)) * (DM * 2) + cg * 128 + (lane & 15) * 8, 0, OAUX);
        }
        STAMP(6);
    } else {
        if (prod) {
            const __amdgpu_buffer_rsrc_t rw = __builtin_amdgcn_make_buffer_rsrc((void*)(mat ? wu : wg), 0, DM * DF * 2, 0x00020000);
            const __amdgpu_buffer_rsrc_t rx = __builtin_amdgcn_make_buffer_rsrc((void*)x, 0, 32 * DM * 2, 0x00020000);
            const int wo = (128 * w + 8 * (lane >> 4)) * (DF * 2) + tile * 64 + (lane & 15) * 4;
            const int xo = (lane & 15) * (DM * 2) + (128 * w + 8 * (lane >> 4)) * 2;
            unsigned a[32];
            v4u xf[2][4];
#pragma unroll
            for (int i = 0; i < 16; ++i) a[i] = __builtin_amdgcn_raw_buffer_load_b32(rw, wo, ((i >> 3) * 32 + (i & 7)) * (DF * 2), WAUX);
#pragma unroll
            for (int ks = 0; ks < 4; ++ks)
#pragma unroll
                for (int tt = 0; tt < 2; ++tt) xf[tt][ks] = __builtin_amdgcn_raw_buffer_load_b128(rx, xo + ks * 64, tt * 16 * (DM * 2), 0);
#pragma unroll
            for (int i = 16; i < 32; ++i) a[i] = __builtin_amdgcn_raw_buffer_load_b32(rw, wo, ((i >> 3) * 32 + (i & 7)) * (DF * 2), WAUX);
            __builtin_amdgcn_sched_barrier(0);
            STAMP(1);
            f32x4 acc[2][2] = {{{0.f, 0.f, 0.f, 0.f}, {0.f, 0.f, 0.f, 0.f}}, {{0.f, 0.f, 0.f, 0.f}, {0.f, 0.f, 0.f, 0.f}}};
#pragma unroll
            for (int ks = 0; ks < 4; ++ks) {
                v4u fe, fo;
#pragma unroll
                for (int j = 0; j < 4; ++j) {
                    fe[j] = __builtin_amdgcn_perm(a[8 * ks + 2 * j + 1], a[8 * ks + 2 * j], 0x05040100u);
                    fo[j] = __builtin_amdgcn_perm(a[8 * ks + 2 * j + 1], a[8 * ks + 2 * j], 0x07060302u);
                }
#pragma unroll
                for (int tt = 0; tt < 2; ++tt) {
                    acc[tt][0] = mfma(fe, xf[tt][ks], acc[tt][0]);
                    acc[tt][1] = mfma(fo, xf[tt][ks], acc[tt][1]);
                }
                __builtin_amdgcn_sched_barrier(0);
            }
#pragma unroll
            for (int tt = 0; tt < 2; ++tt)
#pragma unroll
                for (int par = 0; par < 2; ++par) *(LAS f32x4*)(L + L_RED + w * RED_W + tt * RED_TT + par * RED_PAR + lane * 16) = acc[tt][par];
            STAMP(2);
        }
        BARRIER();
        if (prod) {
            const int u = tid >> 2, i = tid & 3;
            const int ro = L_RED + (u >> 6) * RED_TT + (i >> 1) * RED_PAR + (u & 63) * 16 + (i & 1) * 8;
            f32x2 v = *(LAS f32x2*)(L + ro);
#pragma unroll
            for (int ww = 1; ww < 8; ++ww) v += *(LAS f32x2*)(L + ro + ww * RED_W);
            unsigned d = pack_bf16(v.x, v.y);
            if (mat == 0) d = pack_bf16(gelu_tanh(lo_f(d)), gelu_tanh(hi_f(d)));
            if (d == 0xffffffffu) d = 0x7fffffffu;
            v4u q;
            q.x = d;
            q.y = __builtin_amdgcn_update_dpp(0, (int)d, 0x55, 0xf, 0xf, true);
            q.z = __builtin_amdgcn_update_dpp(0, (int)d, 0xaa, 0xf, 0xf, true);
            q.w = __builtin_amdgcn_update_dpp(0, (int)d, 0xff, 0xf, 0xf, true);
            if (i == 0) __builtin_amdgcn_raw_buffer_store_b128(q, rws, WS_H + (mat * 112 + tile) * 2048 + u * 16, 0, SC1);
        }
        STAMP(3);
        const v2u tagv = *(LAS v2u*)(L + L_TAG);
        const int tt = w >> 2, c16 = w & 3;
        const int po = WS_PART + ((cg * 16 + 8 * tt + ((lane & 15) >> 1)) * 16 + s) * 512 + (lane & 1) * 256 + c16 * 64 + (lane >> 4) * 16;
        const int ho = WS_H + (7 * s + (w < 7 ? w : 0)) * 2048 + lane * 16;
        v4u ge[2], uu[2];
        if (w < 7) {
            ge[0] = __builtin_amdgcn_raw_buffer_load_b128(rws, ho, 0, SC1V);
            ge[1] = __builtin_amdgcn_raw_buffer_load_b128(rws, ho, 1024, SC1V);
            uu[0] = __builtin_amdgcn_raw_buffer_load_b128(rws, ho, 112 * 2048, SC1V);
            uu[1] = __builtin_amdgcn_raw_buffer_load_b128(rws, ho, 112 * 2048 + 1024, SC1V);
        } else if (blockIdx.x == 255) {
#pragma unroll 1
            for (unsigned spin = 0; spin < SPIN_CAP; ++spin) {
                asm volatile("" ::: "memory");
                bool ok = true;
#pragma unroll
                for (int i = 0; i < 4; ++i) {
                    const v2u f = __builtin_amdgcn_raw_buffer_load_b64(rws, WS_FLAG + lane * 128, i * 64 * 128, SC1V);
                    ok &= f.x == tagv.x && f.y == tagv.y;
                }
                if (__all(ok)) break;
                __builtin_amdgcn_s_sleep(4);
            }
            if (lane == 0) __builtin_amdgcn_raw_buffer_store_b64(tagv, rws, WS_EPOCH, 0, SC1);
        }
        __builtin_amdgcn_sched_barrier(0);
        BARRIER();
        v4u wf[7];
        {
            const int kg = lane >> 4, q = (lane & 15) >> 2, p = lane & 3;
            const int f = ((kg & 1) << 1) | (q >> 1);
            const int ao = L_WD + kg * 1024 + q * 128 + (((2 * c16 + (p >> 1)) ^ (f << 1)) << 4) + (p & 1) * 8;
#pragma unroll
            for (int ks = 0; ks < 7; ++ks) {
                const v2u lo = __builtin_bit_cast(v2u, __builtin_amdgcn_ds_read_tr16_b64_v4i16((LAS s16x4*)(L + ao + ks * 4096)));
                const v2u hi = __builtin_bit_cast(v2u, __builtin_amdgcn_ds_read_tr16_b64_v4i16((LAS s16x4*)(L + ao + ks * 4096 + 512)));
                wf[ks] = v4u{lo.x, lo.y, hi.x, hi.y};
            }
        }
        __builtin_amdgcn_sched_barrier(0);
        STAMP(4);
        if (w < 7) {
#pragma unroll 1
            for (unsigned spin = 0; spin < SPIN_CAP; ++spin) {
                bool ok = true;
#pragma unroll
                for (int j = 0; j < 4; ++j) ok &= ge[0][j] != 0xffffffffu && ge[1][j] != 0xffffffffu && uu[0][j] != 0xffffffffu && uu[1][j] != 0xffffffffu;
                if (__all(ok)) break;
                asm volatile("" ::: "memory");
                ge[0] = __builtin_amdgcn_raw_buffer_load_b128(rws, ho, 0, SC1V);
                ge[1] = __builtin_amdgcn_raw_buffer_load_b128(rws, ho, 1024, SC1V);
                uu[0] = __builtin_amdgcn_raw_buffer_load_b128(rws, ho, 112 * 2048, SC1V);
                uu[1] = __builtin_amdgcn_raw_buffer_load_b128(rws, ho, 112 * 2048 + 1024, SC1V);
            }
            STAMP(5);
#pragma unroll
            for (int t2 = 0; t2 < 2; ++t2) {
                v4u h;
#pragma unroll
                for (int j = 0; j < 4; ++j) h[j] = pack_bf16(lo_f(ge[t2][j]) * lo_f(uu[t2][j]), hi_f(ge[t2][j]) * hi_f(uu[t2][j]));
                *(LAS v4u*)(L + L_H + (w * 2 + t2) * 1024 + lane * 16) = h;
            }
        }
        BARRIER();
        f32x4 acc = {0.f, 0.f, 0.f, 0.f};
#pragma unroll
        for (int ks = 0; ks < 7; ++ks) acc = mfma(wf[ks], *(LAS v4u*)(L + L_H + (ks * 2 + tt) * 1024 + lane * 16), acc);
        const unsigned t8 = tagv.x & 0xffu;
        const v4u pv = (__builtin_bit_cast(v4u, acc) & 0xffffff00u) | t8;
        __builtin_amdgcn_raw_buffer_store_b128(pv, rws, po, 0, SC1);
        STAMP(6);
    }
}

extern "C" void kernel_launch(void* const* d_in, const int* in_sizes, int n_in, void* d_out, int out_size, void* d_ws, size_t ws_size,
                              hipStream_t stream) {
    (void)hipFuncSetAttribute((const void*)geglu_kernel, hipFuncAttributeMaxDynamicSharedMemorySize, L_BYTES);
    geglu_kernel<<<256, 576, L_BYTES, stream>>>(d_in[0], d_in[1], d_in[2], d_in[3], d_out, d_ws);
}
```

```cpp
#define PADK 0
#define PADW 0
#include <hip/hip_runtime.h>
#include <stdint.h>

#define WAUX 0
#define DAUX 0
#define OAUX 0
#define PREWC 0
#define CDELAY 8

#define LAS __attribute__((address_space(3)))
typedef unsigned v4u __attribute__((ext_vector_type(4)));
typedef unsigned v2u __attribute__((ext_vector_type(2)));
typedef float f32x4 __attribute__((ext_vector_type(4)));
typedef float f32x2 __attribute__((ext_vector_type(2)));
typedef short s16x8 __attribute__((ext_vector_type(8)));
typedef short s16x4 __attribute__((ext_vector_type(4)));
typedef __bf16 bf16x8 __attribute__((ext_vector_type(8)));
typedef __bf16 bf16x2 __attribute__((ext_vector_type(2)));
typedef unsigned long long u64;

constexpr int DM = 1024, DF = 3584;
constexpr int SC1 = 16;
constexpr int SC1V = 16 | (1 << 31);
constexpr int WS_EPOCH = 0, WS_FLAG = 4096, WS_H = 65536, WS_PART = 1 << 20, WS_BYTES = WS_PART + 16 * 16 * 16 * 512;
constexpr int L_RED = 0, RED_PAR = 1024 + 128, RED_TT = 2 * RED_PAR, RED_W = 2 * RED_TT;
constexpr int L_WD = 8 * RED_W;
constexpr int L_H = L_WD + 28 * 1024;
constexpr int L_TAG = L_H + 14 * 1024;
constexpr int L_BYTES = 84 * 1024;
static_assert(L_TAG + 16 <= L_BYTES, "lds");
constexpr unsigned SPIN_CAP = 1u << 16;

constexpr int WS_STAMP = 8 << 20;
#define STAMP(i) do { __builtin_amdgcn_sched_barrier(0); stamp[i] = __builtin_amdgcn_s_memrealtime(); __builtin_amdgcn_sched_barrier(0); } while (0)

__device__ __forceinline__ f32x4 mfma(v4u a, v4u b, f32x4 c) {
    return __builtin_amdgcn_mfma_f32_16x16x32_bf16(__builtin_bit_cast(bf16x8, a), __builtin_bit_cast(bf16x8, b), c, 0, 0, 0);
}
__device__ __forceinline__ unsigned pack_bf16(float lo, float hi) {
    bf16x2 r; r.x = (__bf16)lo; r.y = (__bf16)hi; return __builtin_bit_cast(unsigned, r);
}
__device__ __forceinline__ float lo_f(unsigned d) { return __builtin_bit_cast(float, d << 16); }
__device__ __forceinline__ float hi_f(unsigned d) { return __builtin_bit_cast(float, d & 0xffff0000u); }
__device__ __forceinline__ float gelu_tanh(float g) {
    const float y2 = g * (2.0f * 0.7978845608028654f * 1.4426950408889634f) * (1.0f + 0.044715f * g * g);
    return g * __builtin_amdgcn_rcpf(1.0f + __builtin_amdgcn_exp2f(-y2));
}
#define BARRIER() do { asm volatile("s_waitcnt lgkmcnt(0)" ::: "memory"); __builtin_amdgcn_s_barrier(); asm volatile("" ::: "memory"); } while (0)

__global__ void __launch_bounds__(576, 1) geglu_kernel(const void* __restrict__ x, const void* __restrict__ wg, const void* __restrict__ wu,
                                                      const void* __restrict__ wd, void* __restrict__ out, void* ws) {
    extern __shared__ __attribute__((aligned(16))) unsigned char smem[];
    LAS unsigned char* L = (LAS unsigned char*)smem;
    u64 stamp[8] = {0, 0, 0, 0, 0, 0, 0, 0};
    STAMP(0);
    asm volatile("" :: "s"(x), "s"(wg), "s"(wu), "s"(wd), "s"(out), "s"(ws));
    const int tid = threadIdx.x, lane = tid & 63, w = __builtin_amdgcn_readfirstlane(tid >> 6);
    const int xl = blockIdx.x & 7, li = blockIdx.x >> 3;
    const bool prod = li < 28;
    const int mat = li & 1, tile = 14 * xl + (li >> 1);
    const int s = 2 * xl + (li >> 4), cg = li & 15;
    const __amdgpu_buffer_rsrc_t rws = __builtin_amdgcn_make_buffer_rsrc(ws, 0, WS_BYTES, 0x00020000);

    if (w == 8) {
        if (prod) {
            const int ho = WS_H + (mat * 112 + tile) * 2048 + lane * 16;
            const v4u ones = {0xffffffffu, 0xffffffffu, 0xffffffffu, 0xffffffffu};
            __builtin_amdgcn_raw_buffer_store_b128(ones, rws, ho, 0, SC1);
            __builtin_amdgcn_raw_buffer_store_b128(ones, rws, ho + 1024, 0, SC1);
        }
        v2u ep = __builtin_amdgcn_raw_buffer_load_b64(rws, WS_EPOCH, 0, SC1);
        asm volatile("s_waitcnt vmcnt(0)" : "+v"(ep) :: "memory");
        const unsigned elo = __builtin_amdgcn_readfirstlane(ep.x), ehi = __builtin_amdgcn_readfirstlane(ep.y);
        const u64 tag = (((u64)ehi << 32) | elo) + 1u;
        const v2u tagv = {(unsigned)tag, (unsigned)(tag >> 32)};
        if (lane == 0) __builtin_amdgcn_raw_buffer_store_b64(tagv, rws, WS_FLAG + (xl * 32 + li) * 128, 0, SC1);
        STAMP(1);
        {
            const int pl = lane < 14 ? lane : 0;
            const int fo = WS_FLAG + (xl * 32 + 14 * (li >> 4) + pl) * 128;
#pragma unroll 1
            for (unsigned spin = 0; spin < SPIN_CAP; ++spin) {
                asm volatile("" ::: "memory");
                const v2u f = __builtin_amdgcn_raw_buffer_load_b64(rws, fo, 0, SC1V);
                if (__all(f.x == tagv.x && f.y == tagv.y)) break;
                __builtin_amdgcn_s_sleep(2);
            }
        }
        STAMP(2);
        if (lane == 0) *(LAS v2u*)(L + L_TAG) = tagv;
        {
            const __amdgpu_buffer_rsrc_t rd = __builtin_amdgcn_make_buffer_rsrc((void*)wd, 0, DM * DF * 2, 0x00020000);
            const int rho = lane >> 3, cpos = lane & 7;
            const int srow = ((rho & 3) << 1) | (rho >> 2);
            const int f0 = (rho >> 1) & 1;
            const int vb = (s * 224 + srow) * (DM * 2) + cg * 128;
            const int v0 = vb + ((cpos ^ (f0 << 1)) << 4), v1 = vb + ((cpos ^ ((f0 | 2) << 1)) << 4);
#pragma unroll
            for (int g = 0; g < 28; ++g)
                __builtin_amdgcn_raw_ptr_buffer_load_lds(rd, (LAS void*)(L + L_WD + g * 1024), 16, (g & 1) ? v1 : v0, g * 8 * (DM * 2), 0, DAUX);
        }
        BARRIER();
        asm volatile("s_waitcnt vmcnt(0)" ::: "memory");
        STAMP(3);
        BARRIER();
        BARRIER();
        STAMP(4);
        for (int i = 0; i < CDELAY; ++i) __builtin_amdgcn_s_sleep(1);
        const int io = WS_PART + (cg * 16 + s) * 8192 + lane * 16;
        const unsigned t8 = tagv.x & 0xffu;
        v4u p[8];
#pragma unroll 1
        for (unsigned spin = 0; spin < SPIN_CAP; ++spin) {
            asm volatile("" ::: "memory");
#pragma unroll
            for (int i = 0; i < 8; ++i) p[i] = __builtin_amdgcn_raw_buffer_load_b128(rws, io, i * 1024, SC1V);
            unsigned bad = 0;
#pragma unroll
            for (int i = 0; i < 8; ++i) bad |= (p[i].x ^ t8) | (p[i].y ^ t8) | (p[i].z ^ t8) | (p[i].w ^ t8);
            if (__all((bad & 0xffu) == 0)) break;
        }
        STAMP(5);
        f32x4 acc = {0.f, 0.f, 0.f, 0.f};
#pragma unroll
        for (int i = 0; i < 8; ++i) acc += __builtin_bit_cast(f32x4, p[i] & 0xffffff00u);
        f32x4 o;
#pragma unroll
        for (int j = 0; j < 4; ++j) o[j] = acc[j] + __shfl_xor(acc[j], 32);
        if (lane < 32) {
            const __amdgpu_buffer_rsrc_t ro = __builtin_amdgcn_make_buffer_rsrc(out, 0, 32 * DM * 2, 0x00020000);
            const v2u ov = {pack_bf16(o[0], o[1]), pack_bf16(o[2], o[3])};
            __builtin_amdgcn_raw_buffer_store_b64(ov, ro, (2 * s + (lane >> 4)) * (DM * 2) + cg * 128 + (lane & 15) * 8, 0, OAUX);
        }
        STAMP(6);
    } else {
        if (prod) {
            const __amdgpu_buffer_rsrc_t rw = __builtin_amdgcn_make_buffer_rsrc((void*)(mat ? wu : wg), 0, DM * DF * 2, 0x00020000);
            const __amdgpu_buffer_rsrc_t rx = __builtin_amdgcn_make_buffer_rsrc((void*)x, 0, 32 * DM * 2, 0x00020000);
            const int wo = (128 * w + 8 * (lane >> 4)) * (DF * 2) + tile * 64 + (lane & 15) * 4;
            const int xo = (lane & 15) * (DM * 2) + (128 * w + 8 * (lane >> 4)) * 2;
            unsigned a[32];
            v4u xf[2][4];
#pragma unroll
            for (int i = 0; i < 16; ++i) a[i] = __builtin_amdgcn_raw_buffer_load_b32(rw, wo, ((i >> 3) * 32 + (i & 7)) * (DF * 2), WAUX);
#pragma unroll
            for (int ks = 0; ks < 4; ++ks)
#pragma unroll
                for (int tt = 0; tt < 2; ++tt) xf[tt][ks] = __builtin_amdgcn_raw_buffer_load_b128(rx, xo + ks * 64, tt * 16 * (DM * 2), 0);
#pragma unroll
            for (int i = 16; i < 32; ++i) a[i] = __builtin_amdgcn_raw_buffer_load_b32(rw, wo, ((i >> 3) * 32 + (i & 7)) * (DF * 2), WAUX);
            __builtin_amdgcn_sched_barrier(0);
            STAMP(1);
            f32x4 acc[2][2] = {{{0.f, 0.f, 0.f, 0.f}, {0.f, 0.f, 0.f, 0.f}}, {{0.f, 0.f, 0.f, 0.f}, {0.f, 0.f, 0.f, 0.f}}};
#pragma unroll
            for (int ks = 0; ks < 4; ++ks) {
                v4u fe, fo;
#pragma unroll
                for (int j = 0; j < 4; ++j) {
                    fe[j] = __builtin_amdgcn_perm(a[8 * ks + 2 * j + 1], a[8 * ks + 2 * j], 0x05040100u);
                    fo[j] = __builtin_amdgcn_perm(a[8 * ks + 2 * j + 1], a[8 * ks + 2 * j], 0x07060302u);
                }
#pragma unroll
                for (int tt = 0; tt < 2; ++tt) {
                    acc[tt][0] = mfma(fe, xf[tt][ks], acc[tt][0]);
                    acc[tt][1] = mfma(fo, xf[tt][ks], acc[tt][1]);
                }
                __builtin_amdgcn_sched_barrier(0);
            }
#pragma unroll
            for (int tt = 0; tt < 2; ++tt)
#pragma unroll
                for (int par = 0; par < 2; ++par) *(LAS f32x4*)(L + L_RED + w * RED_W + tt * RED_TT + par * RED_PAR + lane * 16) = acc[tt][par];
            STAMP(2);
        }
        BARRIER();
        if (prod) {
            const int u = tid >> 2, i = tid & 3;
            const int ro = L_RED + (u >> 6) * RED_TT + (i >> 1) * RED_PAR + (u & 63) * 16 + (i & 1) * 8;
            f32x2 v = *(LAS f32x2*)(L + ro);
#pragma unroll
            for (int ww = 1; ww < 8; ++ww) v += *(LAS f32x2*)(L + ro + ww * RED_W);
            unsigned d = pack_bf16(v.x, v.y);
            if (mat == 0) d = pack_bf16(gelu_tanh(lo_f(d)), gelu_tanh(hi_f(d)));
            if (d == 0xffffffffu) d = 0x7fffffffu;
            v4u q;
            q.x = d;
            q.y = __builtin_amdgcn_update_dpp(0, (int)d, 0x55, 0xf, 0xf, true);
            q.z = __builtin_amdgcn_update_dpp(0, (int)d, 0xaa, 0xf, 0xf, true);
            q.w = __builtin_amdgcn_update_dpp(0, (int)d, 0xff, 0xf, 0xf, true);
            if (i == 0) __builtin_amdgcn_raw_buffer_store_b128(q, rws, WS_H + (mat * 112 + tile) * 2048 + u * 16, 0, SC1);
        }
        STAMP(3);
        const v2u tagv = *(LAS v2u*)(L + L_TAG);
        const int tt = w >> 2, c16 = w & 3;
        const int po = WS_PART + ((cg * 16 + 8 * tt + ((lane & 15) >> 1)) * 16 + s) * 512 + (lane & 1) * 256 + c16 * 64 + (lane >> 4) * 16;
        const int ho = WS_H + (7 * s + (w < 7 ? w : 0)) * 2048 + lane * 16;
        v4u ge[2], uu[2];
        if (w < 7) {
            ge[0] = __builtin_amdgcn_raw_buffer_load_b128(rws, ho, 0, SC1V);
            ge[1] = __builtin_amdgcn_raw_buffer_load_b128(rws, ho, 1024, SC1V);
            uu[0] = __builtin_amdgcn_raw_buffer_load_b128(rws, ho, 112 * 2048, SC1V);
            uu[1] = __builtin_amdgcn_raw_buffer_load_b128(rws, ho, 112 * 2048 + 1024, SC1V);
        } else if (blockIdx.x == 255) {
#pragma unroll 1
            for (unsigned spin = 0; spin < SPIN_CAP; ++spin) {
                asm volatile("" ::: "memory");
                bool ok = true;
#pragma unroll
                for (int i = 0; i < 4; ++i) {
                    const v2u f = __builtin_amdgcn_raw_buffer_load_b64(rws, WS_FLAG + lane * 128, i * 64 * 128, SC1V);
                    ok &= f.x == tagv.x && f.y == tagv.y;
                }
                if (__all(ok)) break;
                __builtin_amdgcn_s_sleep(4);
            }
            if (lane == 0) __builtin_amdgcn_raw_buffer_store_b64(tagv, rws, WS_EPOCH, 0, SC1);
        }
        __builtin_amdgcn_sched_barrier(0);
        BARRIER();
        v4u wf[7];
        {
            const int kg = lane >> 4, q = (lane & 15) >> 2, p = lane & 3;
            const int f = ((kg & 1) << 1) | (q >> 1);
            const int ao = L_WD + kg * 1024 + q * 128 + (((2 * c16 + (p >> 1)) ^ (f << 1)) << 4) + (p & 1) * 8;
#pragma unroll
            for (int ks = 0; ks < 7; ++ks) {
                const v2u lo = __builtin_bit_cast(v2u, __builtin_amdgcn_ds_read_tr16_b64_v4i16((LAS s16x4*)(L + ao + ks * 4096)));
                const v2u hi = __builtin_bit_cast(v2u, __builtin_amdgcn_ds_read_tr16_b64_v4i16((LAS s16x4*)(L + ao + ks * 4096 + 512)));
                wf[ks] = v4u{lo.x, lo.y, hi.x, hi.y};
            }
        }
        __builtin_amdgcn_sched_barrier(0);
        STAMP(4);
        if (w < 7) {
#pragma unroll 1
            for (unsigned spin = 0; spin < SPIN_CAP; ++spin) {
                bool ok = true;
#pragma unroll
                for (int j = 0; j < 4; ++j) ok &= ge[0][j] != 0xffffffffu && ge[1][j] != 0xffffffffu && uu[0][j] != 0xffffffffu && uu[1][j] != 0xffffffffu;
                if (__all(ok)) break;
                asm volatile("" ::: "memory");
                ge[0] = __builtin_amdgcn_raw_buffer_load_b128(rws, ho, 0, SC1V);
                ge[1] = __builtin_amdgcn_raw_buffer_load_b128(rws, ho, 1024, SC1V);
                uu[0] = __builtin_amdgcn_raw_buffer_load_b128(rws, ho, 112 * 2048, SC1V);
                uu[1] = __builtin_amdgcn_raw_buffer_load_b128(rws, ho, 112 * 2048 + 1024, SC1V);
            }
            STAMP(5);
#pragma unroll
            for (int t2 = 0; t2 < 2; ++t2) {
                v4u h;
#pragma unroll
                for (int j = 0; j < 4; ++j) h[j] = pack_bf16(lo_f(ge[t2][j]) * lo_f(uu[t2][j]), hi_f(ge[t2][j]) * hi_f(uu[t2][j]));
                *(LAS v4u*)(L + L_H + (w * 2 + t2) * 1024 + lane * 16) = h;
            }
        }
        BARRIER();
        f32x4 acc = {0.f, 0.f, 0.f, 0.f};
#pragma unroll
        for (int ks = 0; ks < 7; ++ks) acc = mfma(wf[ks], *(LAS v4u*)(L + L_H + (ks * 2 + tt) * 1024 + lane * 16), acc);
        const unsigned t8 = tagv.x & 0xffu;
        const v4u pv = (__builtin_bit_cast(v4u, acc) & 0xffffff00u) | t8;
        __builtin_amdgcn_raw_buffer_store_b128(pv, rws, po, 0, SC1);
        STAMP(6);
    }
    {
        u64 target = stamp[0] + 3000;
        if (PADK > 0 && ((w == 8) == (PADW == 1)) && stamp[PADK] != 0) target += stamp[PADK] - stamp[0];
        while (__builtin_amdgcn_s_memrealtime() < target) __builtin_amdgcn_s_sleep(1);
    }
}

extern "C" void kernel_launch(void* const* d_in, const int* in_sizes, int n_in, void* d_out, int out_size, void* d_ws, size_t ws_size,
                              hipStream_t stream) {
    (void)hipFuncSetAttribute((const void*)geglu_kernel, hipFuncAttributeMaxDynamicSharedMemorySize, L_BYTES);
    geglu_kernel<<<256, 576, L_BYTES, stream>>>(d_in[0], d_in[1], d_in[2], d_in[3], d_out, d_ws);
}
```

```cpp
#define PG 256
#define PT 64
#define PL 0
#include <hip/hip_runtime.h>
#include <stdint.h>

#define WAUX 0
#define DAUX 0
#define OAUX 0
#define PREWC 0
#define CDELAY 8

#define LAS __attribute__((address_space(3)))
typedef unsigned v4u __attribute__((ext_vector_type(4)));
typedef unsigned v2u __attribute__((ext_vector_type(2)));
typedef float f32x4 __attribute__((ext_vector_type(4)));
typedef float f32x2 __attribute__((ext_vector_type(2)));
typedef short s16x8 __attribute__((ext_vector_type(8)));
typedef short s16x4 __attribute__((ext_vector_type(4)));
typedef __bf16 bf16x8 __attribute__((ext_vector_type(8)));
typedef __bf16 bf16x2 __attribute__((ext_vector_type(2)));
typedef unsigned long long u64;

constexpr int DM = 1024, DF = 3584;
constexpr int SC1 = 16;
constexpr int SC1V = 16 | (1 << 31);
constexpr int WS_EPOCH = 0, WS_FLAG = 4096, WS_H = 65536, WS_PART = 1 << 20, WS_BYTES = WS_PART + 16 * 16 * 16 * 512;
constexpr int L_RED = 0, RED_PAR = 1024 + 128, RED_TT = 2 * RED_PAR, RED_W = 2 * RED_TT;
constexpr int L_WD = 8 * RED_W;
constexpr int L_H = L_WD + 28 * 1024;
constexpr int L_TAG = L_H + 14 * 1024;
constexpr int L_BYTES = 84 * 1024;
static_assert(L_TAG + 16 <= L_BYTES, "lds");
constexpr unsigned SPIN_CAP = 1u << 16;

#define STAMP(i) do {} while (0)

__device__ __forceinline__ f32x4 mfma(v4u a, v4u b, f32x4 c) {
    return __builtin_amdgcn_mfma_f32_16x16x32_bf16(__builtin_bit_cast(bf16x8, a), __builtin_bit_cast(bf16x8, b), c, 0, 0, 0);
}
__device__ __forceinline__ unsigned pack_bf16(float lo, float hi) {
    bf16x2 r; r.x = (__bf16)lo; r.y = (__bf16)hi; return __builtin_bit_cast(unsigned, r);
}
__device__ __forceinline__ float lo_f(unsigned d) { return __builtin_bit_cast(float, d << 16); }
__device__ __forceinline__ float hi_f(unsigned d) { return __builtin_bit_cast(float, d & 0xffff0000u); }
__device__ __forceinline__ float gelu_tanh(float g) {
    const float y2 = g * (2.0f * 0.7978845608028654f * 1.4426950408889634f) * (1.0f + 0.044715f * g * g);
    return g * __builtin_amdgcn_rcpf(1.0f + __builtin_amdgcn_exp2f(-y2));
}
#define BARRIER() do { asm volatile("s_waitcnt lgkmcnt(0)" ::: "memory"); __builtin_amdgcn_s_barrier(); asm volatile("" ::: "memory"); } while (0)

__global__ void __launch_bounds__(576, 1) geglu_kernel(const void* __restrict__ x, const void* __restrict__ wg, const void* __restrict__ wu,
                                                      const void* __restrict__ wd, void* __restrict__ out, void* ws) {
    extern __shared__ __attribute__((aligned(16))) unsigned char smem[];
    LAS unsigned char* L = (LAS unsigned char*)smem;
    STAMP(0);
    asm volatile("" :: "s"(x), "s"(wg), "s"(wu), "s"(wd), "s"(out), "s"(ws));
    const int tid = threadIdx.x, lane = tid & 63, w = __builtin_amdgcn_readfirstlane(tid >> 6);
    const int xl = blockIdx.x & 7, li = blockIdx.x >> 3;
    const bool prod = li < 28;
    const int mat = li & 1, tile = 14 * xl + (li >> 1);
    const int s = 2 * xl + (li >> 4), cg = li & 15;
    const __amdgpu_buffer_rsrc_t rws = __builtin_amdgcn_make_buffer_rsrc(ws, 0, WS_BYTES, 0x00020000);

    if (w == 8) {
        if (prod) {
            const int ho = WS_H + (mat * 112 + tile) * 2048 + lane * 16;
            const v4u ones = {0xffffffffu, 0xffffffffu, 0xffffffffu, 0xffffffffu};
            __builtin_amdgcn_raw_buffer_store_b128(ones, rws, ho, 0, SC1);
            __builtin_amdgcn_raw_buffer_store_b128(ones, rws, ho + 1024, 0, SC1);
        }
        v2u ep = __builtin_amdgcn_raw_buffer_load_b64(rws, WS_EPOCH, 0, SC1);
        asm volatile("s_waitcnt vmcnt(0)" : "+v"(ep) :: "memory");
        const unsigned elo = __builtin_amdgcn_readfirstlane(ep.x), ehi = __builtin_amdgcn_readfirstlane(ep.y);
        const u64 tag = (((u64)ehi << 32) | elo) + 1u;
        const v2u tagv = {(unsigned)tag, (unsigned)(tag >> 32)};
        if (lane == 0) __builtin_amdgcn_raw_buffer_store_b64(tagv, rws, WS_FLAG + (xl * 32 + li) * 128, 0, SC1);
        STAMP(1);
        {
            const int pl = lane < 14 ? lane : 0;
            const int fo = WS_FLAG + (xl * 32 + 14 * (li >> 4) + pl) * 128;
#pragma unroll 1
            for (unsigned spin = 0; spin < SPIN_CAP; ++spin) {
                asm volatile("" ::: "memory");
                const v2u f = __builtin_amdgcn_raw_buffer_load_b64(rws, fo, 0, SC1V);
                if (__all(f.x == tagv.x && f.y == tagv.y)) break;
                __builtin_amdgcn_s_sleep(2);
            }
        }
        STAMP(2);
        if (lane == 0) *(LAS v2u*)(L + L_TAG) = tagv;
        {
            const __amdgpu_buffer_rsrc_t rd = __builtin_amdgcn_make_buffer_rsrc((void*)wd, 0, DM * DF * 2, 0x00020000);
            const int rho = lane >> 3, cpos = lane & 7;
            const int srow = ((rho & 3) << 1) | (rho >> 2);
            const int f0 = (rho >> 1) & 1;
            const int vb = (s * 224 + srow) * (DM * 2) + cg * 128;
            const int v0 = vb + ((cpos ^ (f0 << 1)) << 4), v1 = vb + ((cpos ^ ((f0 | 2) << 1)) << 4);
#pragma unroll
            for (int g = 0; g < 28; ++g)
                __builtin_amdgcn_raw_ptr_buffer_load_lds(rd, (LAS void*)(L + L_WD + g * 1024), 16, (g & 1) ? v1 : v0, g * 8 * (DM * 2), 0, DAUX);
        }
        BARRIER();
        asm volatile("s_waitcnt vmcnt(0)" ::: "memory");
        STAMP(3);
        BARRIER();
        BARRIER();
        STAMP(4);
        for (int i = 0; i < CDELAY; ++i) __builtin_amdgcn_s_sleep(1);
        const int io = WS_PART + (cg * 16 + s) * 8192 + lane * 16;
        const unsigned t8 = tagv.x & 0xffu;
        v4u p[8];
#pragma unroll 1
        for (unsigned spin = 0; spin < SPIN_CAP; ++spin) {
            asm volatile("" ::: "memory");
#pragma unroll
            for (int i = 0; i < 8; ++i) p[i] = __builtin_amdgcn_raw_buffer_load_b128(rws, io, i * 1024, SC1V);
            unsigned bad = 0;
#pragma unroll
            for (int i = 0; i < 8; ++i) bad |= (p[i].x ^ t8) | (p[i].y ^ t8) | (p[i].z ^ t8) | (p[i].w ^ t8);
            if (__all((bad & 0xffu) == 0)) break;
        }
        STAMP(5);
        f32x4 acc = {0.f, 0.f, 0.f, 0.f};
#pragma unroll
        for (int i = 0; i < 8; ++i) acc += __builtin_bit_cast(f32x4, p[i] & 0xffffff00u);
        f32x4 o;
#pragma unroll
        for (int j = 0; j < 4; ++j) o[j] = acc[j] + __shfl_xor(acc[j], 32);
        if (lane < 32) {
            const __amdgpu_buffer_rsrc_t ro = __builtin_amdgcn_make_buffer_rsrc(out, 0, 32 * DM * 2, 0x00020000);
            const v2u ov = {pack_bf16(o[0], o[1]), pack_bf16(o[2], o[3])};
            __builtin_amdgcn_raw_buffer_store_b64(ov, ro, (2 * s + (lane >> 4)) * (DM * 2) + cg * 128 + (lane & 15) * 8, 0, OAUX);
        }
        STAMP(6);
    } else {
        if (prod) {
            const __amdgpu_buffer_rsrc_t rw = __builtin_amdgcn_make_buffer_rsrc((void*)(mat ? wu : wg), 0, DM * DF * 2, 0x00020000);
            const __amdgpu_buffer_rsrc_t rx = __builtin_amdgcn_make_buffer_rsrc((void*)x, 0, 32 * DM * 2, 0x00020000);
            const int wo = (128 * w + 8 * (lane >> 4)) * (DF * 2) + tile * 64 + (lane & 15) * 4;
            const int xo = (lane & 15) * (DM * 2) + (128 * w + 8 * (lane >> 4)) * 2;
            unsigned a[32];
            v4u xf[2][4];
#pragma unroll
            for (int i = 0; i < 16; ++i) a[i] = __builtin_amdgcn_raw_buffer_load_b32(rw, wo, ((i >> 3) * 32 + (i & 7)) * (DF * 2), WAUX);
#pragma unroll
            for (int ks = 0; ks < 4; ++ks)
#pragma unroll
                for (int tt = 0; tt < 2; ++tt) xf[tt][ks] = __builtin_amdgcn_raw_buffer_load_b128(rx, xo + ks * 64, tt * 16 * (DM * 2), 0);
#pragma unroll
            for (int i = 16; i < 32; ++i) a[i] = __builtin_amdgcn_raw_buffer_load_b32(rw, wo, ((i >> 3) * 32 + (i & 7)) * (DF * 2), WAUX);
            __builtin_amdgcn_sched_barrier(0);
            STAMP(1);
            f32x4 acc[2][2] = {{{0.f, 0.f, 0.f, 0.f}, {0.f, 0.f, 0.f, 0.f}}, {{0.f, 0.f, 0.f, 0.f}, {0.f, 0.f, 0.f, 0.f}}};
#pragma unroll
            for (int ks = 0; ks < 4; ++ks) {
                v4u fe, fo;
#pragma unroll
                for (int j = 0; j < 4; ++j) {
                    fe[j] = __builtin_amdgcn_perm(a[8 * ks + 2 * j + 1], a[8 * ks + 2 * j], 0x05040100u);
                    fo[j] = __builtin_amdgcn_perm(a[8 * ks + 2 * j + 1], a[8 * ks + 2 * j], 0x07060302u);
                }
#pragma unroll
                for (int tt = 0; tt < 2; ++tt) {
                    acc[tt][0] = mfma(fe, xf[tt][ks], acc[tt][0]);
                    acc[tt][1] = mfma(fo, xf[tt][ks], acc[tt][1]);
                }
                __builtin_amdgcn_sched_barrier(0);
            }
#pragma unroll
            for (int tt = 0; tt < 2; ++tt)
#pragma unroll
                for (int par = 0; par < 2; ++par) *(LAS f32x4*)(L + L_RED + w * RED_W + tt * RED_TT + par * RED_PAR + lane * 16) = acc[tt][par];
            STAMP(2);
        }
        BARRIER();
        if (prod) {
            const int u = tid >> 2, i = tid & 3;
            const int ro = L_RED + (u >> 6) * RED_TT + (i >> 1) * RED_PAR + (u & 63) * 16 + (i & 1) * 8;
            f32x2 v = *(LAS f32x2*)(L + ro);
#pragma unroll
            for (int ww = 1; ww < 8; ++ww) v += *(LAS f32x2*)(L + ro + ww * RED_W);
            unsigned d = pack_bf16(v.x, v.y);
            if (mat == 0) d = pack_bf16(gelu_tanh(lo_f(d)), gelu_tanh(hi_f(d)));
            if (d == 0xffffffffu) d = 0x7fffffffu;
            v4u q;
            q.x = d;
            q.y = __builtin_amdgcn_update_dpp(0, (int)d, 0x55, 0xf, 0xf, true);
            q.z = __builtin_amdgcn_update_dpp(0, (int)d, 0xaa, 0xf, 0xf, true);
            q.w = __builtin_amdgcn_update_dpp(0, (int)d, 0xff, 0xf, 0xf, true);
            if (i == 0) __builtin_amdgcn_raw_buffer_store_b128(q, rws, WS_H + (mat * 112 + tile) * 2048 + u * 16, 0, SC1);
        }
        STAMP(3);
        const v2u tagv = *(LAS v2u*)(L + L_TAG);
        const int tt = w >> 2, c16 = w & 3;
        const int po = WS_PART + ((cg * 16 + 8 * tt + ((lane & 15) >> 1)) * 16 + s) * 512 + (lane & 1) * 256 + c16 * 64 + (lane >> 4) * 16;
        const int ho = WS_H + (7 * s + (w < 7 ? w : 0)) * 2048 + lane * 16;
        v4u ge[2], uu[2];
        if (w < 7) {
            ge[0] = __builtin_amdgcn_raw_buffer_load_b128(rws, ho, 0, SC1V);
            ge[1] = __builtin_amdgcn_raw_buffer_load_b128(rws, ho, 1024, SC1V);
            uu[0] = __builtin_amdgcn_raw_buffer_load_b128(rws, ho, 112 * 2048, SC1V);
            uu[1] = __builtin_amdgcn_raw_buffer_load_b128(rws, ho, 112 * 2048 + 1024, SC1V);
        } else if (blockIdx.x == 255) {
#pragma unroll 1
            for (unsigned spin = 0; spin < SPIN_CAP; ++spin) {
                asm volatile("" ::: "memory");
                bool ok = true;
#pragma unroll
                for (int i = 0; i < 4; ++i) {
                    const v2u f = __builtin_amdgcn_raw_buffer_load_b64(rws, WS_FLAG + lane * 128, i * 64 * 128, SC1V);
                    ok &= f.x == tagv.x && f.y == tagv.y;
                }
                if (__all(ok)) break;
                __builtin_amdgcn_s_sleep(4);
            }
            if (lane == 0) __builtin_amdgcn_raw_buffer_store_b64(tagv, rws, WS_EPOCH, 0, SC1);
        }
        __builtin_amdgcn_sched_barrier(0);
        BARRIER();
        v4u wf[7];
        {
            const int kg = lane >> 4, q = (lane & 15) >> 2, p = lane & 3;
            const int f = ((kg & 1) << 1) | (q >> 1);
            const int ao = L_WD + kg * 1024 + q * 128 + (((2 * c16 + (p >> 1)) ^ (f << 1)) << 4) + (p & 1) * 8;
#pragma unroll
            for (int ks = 0; ks < 7; ++ks) {
                const v2u lo = __builtin_bit_cast(v2u, __builtin_amdgcn_ds_read_tr16_b64_v4i16((LAS s16x4*)(L + ao + ks * 4096)));
                const v2u hi = __builtin_bit_cast(v2u, __builtin_amdgcn_ds_read_tr16_b64_v4i16((LAS s16x4*)(L + ao + ks * 4096 + 512)));
                wf[ks] = v4u{lo.x, lo.y, hi.x, hi.y};
            }
        }
        __builtin_amdgcn_sched_barrier(0);
        STAMP(4);
        if (w < 7) {
#pragma unroll 1
            for (unsigned spin = 0; spin < SPIN_CAP; ++spin) {
                bool ok = true;
#pragma unroll
                for (int j = 0; j < 4; ++j) ok &= ge[0][j] != 0xffffffffu && ge[1][j] != 0xffffffffu && uu[0][j] != 0xffffffffu && uu[1][j] != 0xffffffffu;
                if (__all(ok)) break;
                asm volatile("" ::: "memory");
                ge[0] = __builtin_amdgcn_raw_buffer_load_b128(rws, ho, 0, SC1V);
                ge[1] = __builtin_amdgcn_raw_buffer_load_b128(rws, ho, 1024, SC1V);
                uu[0] = __builtin_amdgcn_raw_buffer_load_b128(rws, ho, 112 * 2048, SC1V);
                uu[1] = __builtin_amdgcn_raw_buffer_load_b128(rws, ho, 112 * 2048 + 1024, SC1V);
            }
            STAMP(5);
#pragma unroll
            for (int t2 = 0; t2 < 2; ++t2) {
                v4u h;
#pragma unroll
                for (int j = 0; j < 4; ++j) h[j] = pack_bf16(lo_f(ge[t2][j]) * lo_f(uu[t2][j]), hi_f(ge[t2][j]) * hi_f(uu[t2][j]));
                *(LAS v4u*)(L + L_H + (w * 2 + t2) * 1024 + lane * 16) = h;
            }
        }
        BARRIER();
        f32x4 acc = {0.f, 0.f, 0.f, 0.f};
#pragma unroll
        for (int ks = 0; ks < 7; ++ks) acc = mfma(wf[ks], *(LAS v4u*)(L + L_H + (ks * 2 + tt) * 1024 + lane * 16), acc);
        const unsigned t8 = tagv.x & 0xffu;
        const v4u pv = (__builtin_bit_cast(v4u, acc) & 0xffffff00u) | t8;
        __builtin_amdgcn_raw_buffer_store_b128(pv, rws, po, 0, SC1);
        STAMP(6);
    }
}

__global__ void __launch_bounds__(PT) probe_kernel(int dummy) {
    const u64 t = __builtin_amdgcn_s_memrealtime() + 3000;
    while (__builtin_amdgcn_s_memrealtime() < t) __builtin_amdgcn_s_sleep(1);
}

extern "C" void kernel_launch(void* const* d_in, const int* in_sizes, int n_in, void* d_out, int out_size, void* d_ws, size_t ws_size,
                              hipStream_t stream) {
    (void)hipFuncSetAttribute((const void*)geglu_kernel, hipFuncAttributeMaxDynamicSharedMemorySize, L_BYTES);
    (void)hipFuncSetAttribute((const void*)probe_kernel, hipFuncAttributeMaxDynamicSharedMemorySize, 160 * 1024);
    probe_kernel<<<PG, PT, PL, stream>>>(0);
    geglu_kernel<<<256, 576, L_BYTES, stream>>>(d_in[0], d_in[1], d_in[2], d_in[3], d_out, d_ws);
}
```

```cpp
#define PADK 0
#define PADW 0
#define PMODE 0
#define PAUX 0
#include <hip/hip_runtime.h>
#include <stdint.h>

#define WAUX 0
#define DAUX 0
#define OAUX 0
#define PREWC 0
#define CDELAY 8

#define LAS __attribute__((address_space(3)))
typedef unsigned v4u __attribute__((ext_vector_type(4)));
typedef unsigned v2u __attribute__((ext_vector_type(2)));
typedef float f32x4 __attribute__((ext_vector_type(4)));
typedef float f32x2 __attribute__((ext_vector_type(2)));
typedef short s16x8 __attribute__((ext_vector_type(8)));
typedef short s16x4 __attribute__((ext_vector_type(4)));
typedef __bf16 bf16x8 __attribute__((ext_vector_type(8)));
typedef __bf16 bf16x2 __attribute__((ext_vector_type(2)));
typedef unsigned long long u64;

constexpr int DM = 1024, DF = 3584;
constexpr int SC1 = 16;
constexpr int SC1V = 16 | (1 << 31);
constexpr int WS_EPOCH = 0, WS_FLAG = 4096, WS_H = 65536, WS_PART = 1 << 20, WS_BYTES = WS_PART + 16 * 16 * 16 * 512;
constexpr int L_RED = 0, RED_PAR = 1024 + 128, RED_TT = 2 * RED_PAR, RED_W = 2 * RED_TT;
constexpr int L_WD = 8 * RED_W;
constexpr int L_H = L_WD + 28 * 1024;
constexpr int L_TAG = L_H + 14 * 1024;
constexpr int L_BYTES = 84 * 1024;
static_assert(L_TAG + 16 <= L_BYTES, "lds");
constexpr unsigned SPIN_CAP = 1u << 16;

constexpr int WS_STAMP = 8 << 20;
#define STAMP(i) do { __builtin_amdgcn_sched_barrier(0); stamp[i] = __builtin_amdgcn_s_memrealtime(); __builtin_amdgcn_sched_barrier(0); } while (0)

__device__ __forceinline__ f32x4 mfma(v4u a, v4u b, f32x4 c) {
    return __builtin_amdgcn_mfma_f32_16x16x32_bf16(__builtin_bit_cast(bf16x8, a), __builtin_bit_cast(bf16x8, b), c, 0, 0, 0);
}
__device__ __forceinline__ unsigned pack_bf16(float lo, float hi) {
    bf16x2 r; r.x = (__bf16)lo; r.y = (__bf16)hi; return __builtin_bit_cast(unsigned, r);
}
__device__ __forceinline__ float lo_f(unsigned d) { return __builtin_bit_cast(float, d << 16); }
__device__ __forceinline__ float hi_f(unsigned d) { return __builtin_bit_cast(float, d & 0xffff0000u); }
__device__ __forceinline__ float gelu_tanh(float g) {
    const float y2 = g * (2.0f * 0.7978845608028654f * 1.4426950408889634f) * (1.0f + 0.044715f * g * g);
    return g * __builtin_amdgcn_rcpf(1.0f + __builtin_amdgcn_exp2f(-y2));
}
#define BARRIER() do { asm volatile("s_waitcnt lgkmcnt(0)" ::: "memory"); __builtin_amdgcn_s_barrier(); asm volatile("" ::: "memory"); } while (0)

__global__ void __launch_bounds__(576, 1) geglu_kernel(const void* __restrict__ x, const void* __restrict__ wg, const void* __restrict__ wu,
                                                      const void* __restrict__ wd, void* __restrict__ out, void* ws) {
    extern __shared__ __attribute__((aligned(16))) unsigned char smem[];
    LAS unsigned char* L = (LAS unsigned char*)smem;
    u64 stamp[8] = {0, 0, 0, 0, 0, 0, 0, 0};
    STAMP(0);
    asm volatile("" :: "s"(x), "s"(wg), "s"(wu), "s"(wd), "s"(out), "s"(ws));
    const int tid = threadIdx.x, lane = tid & 63, w = __builtin_amdgcn_readfirstlane(tid >> 6);
    const int xl = blockIdx.x & 7, li = blockIdx.x >> 3;
    const bool prod = li < 28;
    const int mat = li & 1, tile = 14 * xl + (li >> 1);
    const int s = 2 * xl + (li >> 4), cg = li & 15;
    const __amdgpu_buffer_rsrc_t rws = __builtin_amdgcn_make_buffer_rsrc(ws, 0, WS_BYTES, 0x00020000);

    if (w == 8) {
        if (prod) {
            const int ho = WS_H + (mat * 112 + tile) * 2048 + lane * 16;
            const v4u ones = {0xffffffffu, 0xffffffffu, 0xffffffffu, 0xffffffffu};
            __builtin_amdgcn_raw_buffer_store_b128(ones, rws, ho, 0, SC1);
            __builtin_amdgcn_raw_buffer_store_b128(ones, rws, ho + 1024, 0, SC1);
        }
        v2u ep = __builtin_amdgcn_raw_buffer_load_b64(rws, WS_EPOCH, 0, SC1);
        asm volatile("s_waitcnt vmcnt(0)" : "+v"(ep) :: "memory");
        const unsigned elo = __builtin_amdgcn_readfirstlane(ep.x), ehi = __builtin_amdgcn_readfirstlane(ep.y);
        const u64 tag = (((u64)ehi << 32) | elo) + 1u;
        const v2u tagv = {(unsigned)tag, (unsigned)(tag >> 32)};
        if (lane == 0) __builtin_amdgcn_raw_buffer_store_b64(tagv, rws, WS_FLAG + (xl * 32 + li) * 128, 0, SC1);
        STAMP(1);
        {
            const int pl = lane < 14 ? lane : 0;
            const int fo = WS_FLAG + (xl * 32 + 14 * (li >> 4) + pl) * 128;
#pragma unroll 1
            for (unsigned spin = 0; spin < SPIN_CAP; ++spin) {
                asm volatile("" ::: "memory");
                const v2u f = __builtin_amdgcn_raw_buffer_load_b64(rws, fo, 0, SC1V);
                if (__all(f.x == tagv.x && f.y == tagv.y)) break;
                __builtin_amdgcn_s_sleep(2);
            }
        }
        STAMP(2);
        if (lane == 0) *(LAS v2u*)(L + L_TAG) = tagv;
        {
            const __amdgpu_buffer_rsrc_t rd = __builtin_amdgcn_make_buffer_rsrc((void*)wd, 0, DM * DF * 2, 0x00020000);
            const int rho = lane >> 3, cpos = lane & 7;
            const int srow = ((rho & 3) << 1) | (rho >> 2);
            const int f0 = (rho >> 1) & 1;
            const int vb = (s * 224 + srow) * (DM * 2) + cg * 128;
            const int v0 = vb + ((cpos ^ (f0 << 1)) << 4), v1 = vb + ((cpos ^ ((f0 | 2) << 1)) << 4);
#pragma unroll
            for (int g = 0; g < 28; ++g)
                __builtin_amdgcn_raw_ptr_buffer_load_lds(rd, (LAS void*)(L + L_WD + g * 1024), 16, (g & 1) ? v1 : v0, g * 8 * (DM * 2), 0, DAUX);
        }
        BARRIER();
        asm volatile("s_waitcnt vmcnt(0)" ::: "memory");
        STAMP(3);
        BARRIER();
        BARRIER();
        STAMP(4);
        for (int i = 0; i < CDELAY; ++i) __builtin_amdgcn_s_sleep(1);
        const int io = WS_PART + (cg * 16 + s) * 8192 + lane * 16;
        const unsigned t8 = tagv.x & 0xffu;
        v4u p[8];
#pragma unroll 1
        for (unsigned spin = 0; spin < SPIN_CAP; ++spin) {
            asm volatile("" ::: "memory");
#pragma unroll
            for (int i = 0; i < 8; ++i) p[i] = __builtin_amdgcn_raw_buffer_load_b128(rws, io, i * 1024, SC1V);
            unsigned bad = 0;
#pragma unroll
            for (int i = 0; i < 8; ++i) bad |= (p[i].x ^ t8) | (p[i].y ^ t8) | (p[i].z ^ t8) | (p[i].w ^ t8);
            if (__all((bad & 0xffu) == 0)) break;
        }
        STAMP(5);
        f32x4 acc = {0.f, 0.f, 0.f, 0.f};
#pragma unroll
        for (int i = 0; i < 8; ++i) acc += __builtin_bit_cast(f32x4, p[i] & 0xffffff00u);
        f32x4 o;
#pragma unroll
        for (int j = 0; j < 4; ++j) o[j] = acc[j] + __shfl_xor(acc[j], 32);
        if (lane < 32) {
            const __amdgpu_buffer_rsrc_t ro = __builtin_amdgcn_make_buffer_rsrc(out, 0, 32 * DM * 2, 0x00020000);
            const v2u ov = {pack_bf16(o[0], o[1]), pack_bf16(o[2], o[3])};
            __builtin_amdgcn_raw_buffer_store_b64(ov, ro, (2 * s + (lane >> 4)) * (DM * 2) + cg * 128 + (lane & 15) * 8, 0, OAUX);
        }
        STAMP(6);
    } else {
        if (prod) {
            const __amdgpu_buffer_rsrc_t rw = __builtin_amdgcn_make_buffer_rsrc((void*)(mat ? wu : wg), 0, DM * DF * 2, 0x00020000);
            const __amdgpu_buffer_rsrc_t rx = __builtin_amdgcn_make_buffer_rsrc((void*)x, 0, 32 * DM * 2, 0x00020000);
            const int wo = (128 * w + 8 * (lane >> 4)) * (DF * 2) + tile * 64 + (lane & 15) * 4;
            const int xo = (lane & 15) * (DM * 2) + (128 * w + 8 * (lane >> 4)) * 2;
            unsigned a[32];
            v4u xf[2][4];
#pragma unroll
            for (int i = 0; i < 16; ++i) a[i] = __builtin_amdgcn_raw_buffer_load_b32(rw, wo, ((i >> 3) * 32 + (i & 7)) * (DF * 2), WAUX);
#pragma unroll
            for (int ks = 0; ks < 4; ++ks)
#pragma unroll
                for (int tt = 0; tt < 2; ++tt) xf[tt][ks] = __builtin_amdgcn_raw_buffer_load_b128(rx, xo + ks * 64, tt * 16 * (DM * 2), 0);
#pragma unroll
            for (int i = 16; i < 32; ++i) a[i] = __builtin_amdgcn_raw_buffer_load_b32(rw, wo, ((i >> 3) * 32 + (i & 7)) * (DF * 2), WAUX);
            __builtin_amdgcn_sched_barrier(0);
            STAMP(1);
            f32x4 acc[2][2] = {{{0.f, 0.f, 0.f, 0.f}, {0.f, 0.f, 0.f, 0.f}}, {{0.f, 0.f, 0.f, 0.f}, {0.f, 0.f, 0.f, 0.f}}};
#pragma unroll
            for (int ks = 0; ks < 4; ++ks) {
                v4u fe, fo;
#pragma unroll
                for (int j = 0; j < 4; ++j) {
                    fe[j] = __builtin_amdgcn_perm(a[8 * ks + 2 * j + 1], a[8 * ks + 2 * j], 0x05040100u);
                    fo[j] = __builtin_amdgcn_perm(a[8 * ks + 2 * j + 1], a[8 * ks + 2 * j], 0x07060302u);
                }
#pragma unroll
                for (int tt = 0; tt < 2; ++tt) {
                    acc[tt][0] = mfma(fe, xf[tt][ks], acc[tt][0]);
                    acc[tt][1] = mfma(fo, xf[tt][ks], acc[tt][1]);
                }
                __builtin_amdgcn_sched_barrier(0);
            }
#pragma unroll
            for (int tt = 0; tt < 2; ++tt)
#pragma unroll
                for (int par = 0; par < 2; ++par) *(LAS f32x4*)(L + L_RED + w * RED_W + tt * RED_TT + par * RED_PAR + lane * 16) = acc[tt][par];
            STAMP(2);
        }
        BARRIER();
        if (prod) {
            const int u = tid >> 2, i = tid & 3;
            const int ro = L_RED + (u >> 6) * RED_TT + (i >> 1) * RED_PAR + (u & 63) * 16 + (i & 1) * 8;
            f32x2 v = *(LAS f32x2*)(L + ro);
#pragma unroll
            for (int ww = 1; ww < 8; ++ww) v += *(LAS f32x2*)(L + ro + ww * RED_W);
            unsigned d = pack_bf16(v.x, v.y);
            if (mat == 0) d = pack_bf16(gelu_tanh(lo_f(d)), gelu_tanh(hi_f(d)));
            if (d == 0xffffffffu) d = 0x7fffffffu;
            v4u q;
            q.x = d;
            q.y = __builtin_amdgcn_update_dpp(0, (int)d, 0x55, 0xf, 0xf, true);
            q.z = __builtin_amdgcn_update_dpp(0, (int)d, 0xaa, 0xf, 0xf, true);
            q.w = __builtin_amdgcn_update_dpp(0, (int)d, 0xff, 0xf, 0xf, true);
            if (i == 0) __builtin_amdgcn_raw_buffer_store_b128(q, rws, WS_H + (mat * 112 + tile) * 2048 + u * 16, 0, SC1);
        }
        STAMP(3);
        const v2u tagv = *(LAS v2u*)(L + L_TAG);
        const int tt = w >> 2, c16 = w & 3;
        const int po = WS_PART + ((cg * 16 + 8 * tt + ((lane & 15) >> 1)) * 16 + s) * 512 + (lane & 1) * 256 + c16 * 64 + (lane >> 4) * 16;
        const int ho = WS_H + (7 * s + (w < 7 ? w : 0)) * 2048 + lane * 16;
        v4u ge[2], uu[2];
        if (w < 7) {
            ge[0] = __builtin_amdgcn_raw_buffer_load_b128(rws, ho, 0, SC1V);
            ge[1] = __builtin_amdgcn_raw_buffer_load_b128(rws, ho, 1024, SC1V);
            uu[0] = __builtin_amdgcn_raw_buffer_load_b128(rws, ho, 112 * 2048, SC1V);
            uu[1] = __builtin_amdgcn_raw_buffer_load_b128(rws, ho, 112 * 2048 + 1024, SC1V);
        } else if (blockIdx.x == 255) {
#pragma unroll 1
            for (unsigned spin = 0; spin < SPIN_CAP; ++spin) {
                asm volatile("" ::: "memory");
                bool ok = true;
#pragma unroll
                for (int i = 0; i < 4; ++i) {
                    const v2u f = __builtin_amdgcn_raw_buffer_load_b64(rws, WS_FLAG + lane * 128, i * 64 * 128, SC1V);
                    ok &= f.x == tagv.x && f.y == tagv.y;
                }
                if (__all(ok)) break;
                __builtin_amdgcn_s_sleep(4);
            }
            if (lane == 0) __builtin_amdgcn_raw_buffer_store_b64(tagv, rws, WS_EPOCH, 0, SC1);
        }
        __builtin_amdgcn_sched_barrier(0);
        BARRIER();
        v4u wf[7];
        {
            const int kg = lane >> 4, q = (lane & 15) >> 2, p = lane & 3;
            const int f = ((kg & 1) << 1) | (q >> 1);
            const int ao = L_WD + kg * 1024 + q * 128 + (((2 * c16 + (p >> 1)) ^ (f << 1)) << 4) + (p & 1) * 8;
#pragma unroll
            for (int ks = 0; ks < 7; ++ks) {
                const v2u lo = __builtin_bit_cast(v2u, __builtin_amdgcn_ds_read_tr16_b64_v4i16((LAS s16x4*)(L + ao + ks * 4096)));
                const v2u hi = __builtin_bit_cast(v2u, __builtin_amdgcn_ds_read_tr16_b64_v4i16((LAS s16x4*)(L + ao + ks * 4096 + 512)));
                wf[ks] = v4u{lo.x, lo.y, hi.x, hi.y};
            }
        }
        __builtin_amdgcn_sched_barrier(0);
        STAMP(4);
        if (w < 7) {
#pragma unroll 1
            for (unsigned spin = 0; spin < SPIN_CAP; ++spin) {
                bool ok = true;
#pragma unroll
                for (int j = 0; j < 4; ++j) ok &= ge[0][j] != 0xffffffffu && ge[1][j] != 0xffffffffu && uu[0][j] != 0xffffffffu && uu[1][j] != 0xffffffffu;
                if (__all(ok)) break;
                asm volatile("" ::: "memory");
                ge[0] = __builtin_amdgcn_raw_buffer_load_b128(rws, ho, 0, SC1V);
                ge[1] = __builtin_amdgcn_raw_buffer_load_b128(rws, ho, 1024, SC1V);
                uu[0] = __builtin_amdgcn_raw_buffer_load_b128(rws, ho, 112 * 2048, SC1V);
                uu[1] = __builtin_amdgcn_raw_buffer_load_b128(rws, ho, 112 * 2048 + 1024, SC1V);
            }
            STAMP(5);
#pragma unroll
            for (int t2 = 0; t2 < 2; ++t2) {
                v4u h;
#pragma unroll
                for (int j = 0; j < 4; ++j) h[j] = pack_bf16(lo_f(ge[t2][j]) * lo_f(uu[t2][j]), hi_f(ge[t2][j]) * hi_f(uu[t2][j]));
                *(LAS v4u*)(L + L_H + (w * 2 + t2) * 1024 + lane * 16) = h;
            }
        }
        BARRIER();
        f32x4 acc = {0.f, 0.f, 0.f, 0.f};
#pragma unroll
        for (int ks = 0; ks < 7; ++ks) acc = mfma(wf[ks], *(LAS v4u*)(L + L_H + (ks * 2 + tt) * 1024 + lane * 16), acc);
        const unsigned t8 = tagv.x & 0xffu;
        const v4u pv = (__builtin_bit_cast(v4u, acc) & 0xffffff00u) | t8;
        __builtin_amdgcn_raw_buffer_store_b128(pv, rws, po, 0, SC1);
        STAMP(6);
    }
    {
        u64 target = stamp[0] + 3000;
        if (PADK > 0 && ((w == 8) == (PADW == 1)) && stamp[PADK] != 0) target += stamp[PADK] - stamp[0];
        while (__builtin_amdgcn_s_memrealtime() < target) __builtin_amdgcn_s_sleep(1);
    }
}

__global__ void __launch_bounds__(512) probe_kernel(const void* wg, const void* wu, const void* wd, unsigned* sink) {
    const int lane = threadIdx.x & 63, w = __builtin_amdgcn_readfirstlane(threadIdx.x >> 6);
    const int b = blockIdx.x, xl = b & 7, li = b >> 3;
    const __amdgpu_buffer_rsrc_t rg = __builtin_amdgcn_make_buffer_rsrc((void*)wg, 0, DM * DF * 2, 0x00020000);
    const __amdgpu_buffer_rsrc_t ru = __builtin_amdgcn_make_buffer_rsrc((void*)wu, 0, DM * DF * 2, 0x00020000);
    const __amdgpu_buffer_rsrc_t rd = __builtin_amdgcn_make_buffer_rsrc((void*)wd, 0, DM * DF * 2, 0x00020000);
    v4u a[8], d[4];
#pragma unroll
    for (int i = 0; i < 8; ++i) a[i] = v4u{0, 0, 0, 0};
#pragma unroll
    for (int i = 0; i < 4; ++i) d[i] = v4u{0, 0, 0, 0};
    const int id = xl * 28 + li;
    if (li < 28) {
        if (PMODE == 1 || PMODE == 5) {
            const int voff = id * 32768 + w * 4096 + lane * 16;
#pragma unroll
            for (int i = 0; i < 8; ++i) a[i] = __builtin_amdgcn_raw_buffer_load_b128((i & 1) ? ru : rg, voff, (i >> 1) * 1024, PAUX);
        } else if (PMODE == 2 || PMODE == 4) {
            const int ks = id & 3, ct = id >> 2;
            const int voff = (ks * 256 + w * 32 + (lane >> 3)) * (DF * 2) + ct * 128 + (lane & 7) * 16;
#pragma unroll
            for (int i = 0; i < 8; ++i) a[i] = __builtin_amdgcn_raw_buffer_load_b128((i & 1) ? ru : rg, voff, (i >> 1) * 8 * (DF * 2), PAUX);
        } else if (PMODE == 3) {
            const int mat = li & 1, tile = 14 * xl + (li >> 1);
            const int voff = (128 * w + (lane >> 2)) * (DF * 2) + tile * 64 + (lane & 3) * 16;
#pragma unroll
            for (int i = 0; i < 8; ++i) a[i] = __builtin_amdgcn_raw_buffer_load_b128(mat ? ru : rg, voff, i * 16 * (DF * 2), PAUX);
        }
    }
    const int did = xl * 32 + li;
    if (w < 7) {
        if (PMODE == 1) {
#pragma unroll
            for (int i = 0; i < 4; ++i) d[i] = __builtin_amdgcn_raw_buffer_load_b128(rd, did * 28672 + w * 4096 + lane * 16, i * 1024, PAUX);
        } else if (PMODE == 4) {
            const int voff = ((did >> 4) * 224 + w * 32 + (lane >> 3)) * (DM * 2) + (did & 15) * 128 + (lane & 7) * 16;
#pragma unroll
            for (int i = 0; i < 4; ++i) d[i] = __builtin_amdgcn_raw_buffer_load_b128(rd, voff, i * 8 * (DM * 2), PAUX);
        }
    }
    unsigned acc = 0;
#pragma unroll
    for (int i = 0; i < 8; ++i) acc ^= a[i].x ^ a[i].y ^ a[i].z ^ a[i].w;
#pragma unroll
    for (int i = 0; i < 4; ++i) acc ^= d[i].x ^ d[i].y ^ d[i].z ^ d[i].w;
    asm volatile("" :: "v"(acc));
}

extern "C" void kernel_launch(void* const* d_in, const int* in_sizes, int n_in, void* d_out, int out_size, void* d_ws, size_t ws_size,
                              hipStream_t stream) {
    (void)hipFuncSetAttribute((const void*)geglu_kernel, hipFuncAttributeMaxDynamicSharedMemorySize, L_BYTES);
    probe_kernel<<<256, 512, 0, stream>>>(d_in[1], d_in[2], d_in[3], (unsigned*)d_ws);
    geglu_kernel<<<256, 576, L_BYTES, stream>>>(d_in[0], d_in[1], d_in[2], d_in[3], d_out, d_ws);
}
```

```cpp
#define PADK 0
#define PADW 0
#include <hip/hip_runtime.h>
#include <stdint.h>

#define WAUX 0
#define DAUX 0
#define OAUX 0
#define PREWC 0
#define CDELAY 8

#define LAS __attribute__((address_space(3)))
typedef unsigned v4u __attribute__((ext_vector_type(4)));
typedef unsigned v2u __attribute__((ext_vector_type(2)));
typedef float f32x4 __attribute__((ext_vector_type(4)));
typedef float f32x2 __attribute__((ext_vector_type(2)));
typedef short s16x8 __attribute__((ext_vector_type(8)));
typedef short s16x4 __attribute__((ext_vector_type(4)));
typedef __bf16 bf16x8 __attribute__((ext_vector_type(8)));
typedef __bf16 bf16x2 __attribute__((ext_vector_type(2)));
typedef unsigned long long u64;

constexpr int DM = 1024, DF = 3584;
constexpr int SC1 = 16;
constexpr int SC1V = 16 | (1 << 31);
constexpr int WS_EPOCH = 0, WS_FLAG = 4096, WS_H = 65536, WS_PART = 1 << 20, WS_BYTES = WS_PART + 16 * 16 * 16 * 512;
constexpr int L_RED = 0, RED_PAR = 1024 + 128, RED_TT = 2 * RED_PAR, RED_W = 2 * RED_TT;
constexpr int L_WD = 8 * RED_W;
constexpr int L_H = L_WD + 28 * 1024;
constexpr int L_TAG = L_H + 14 * 1024;
constexpr int L_BYTES = 84 * 1024;
static_assert(L_TAG + 16 <= L_BYTES, "lds");
constexpr unsigned SPIN_CAP = 1u << 16;

constexpr int WS_STAMP = 8 << 20;
#define STAMP(i) do { __builtin_amdgcn_sched_barrier(0); stamp[i] = __builtin_amdgcn_s_memrealtime(); __builtin_amdgcn_sched_barrier(0); } while (0)

__device__ __forceinline__ f32x4 mfma(v4u a, v4u b, f32x4 c) {
    return __builtin_amdgcn_mfma_f32_16x16x32_bf16(__builtin_bit_cast(bf16x8, a), __builtin_bit_cast(bf16x8, b), c, 0, 0, 0);
}
__device__ __forceinline__ unsigned pack_bf16(float lo, float hi) {
    bf16x2 r; r.x = (__bf16)lo; r.y = (__bf16)hi; return __builtin_bit_cast(unsigned, r);
}
__device__ __forceinline__ float lo_f(unsigned d) { return __builtin_bit_cast(float, d << 16); }
__device__ __forceinline__ float hi_f(unsigned d) { return __builtin_bit_cast(float, d & 0xffff0000u); }
__device__ __forceinline__ float gelu_tanh(float g) {
    const float y2 = g * (2.0f * 0.7978845608028654f * 1.4426950408889634f) * (1.0f + 0.044715f * g * g);
    return g * __builtin_amdgcn_rcpf(1.0f + __builtin_amdgcn_exp2f(-y2));
}
#define BARRIER() do { asm volatile("s_waitcnt lgkmcnt(0)" ::: "memory"); __builtin_amdgcn_s_barrier(); asm volatile("" ::: "memory"); } while (0)

__global__ void __launch_bounds__(576, 1) geglu_kernel(const void* __restrict__ x, const void* __restrict__ wg, const void* __restrict__ wu,
                                                      const void* __restrict__ wd, void* __restrict__ out, void* ws) {
    extern __shared__ __attribute__((aligned(16))) unsigned char smem[];
    LAS unsigned char* L = (LAS unsigned char*)smem;
    u64 stamp[8] = {0, 0, 0, 0, 0, 0, 0, 0};
    STAMP(0);
    asm volatile("" :: "s"(x), "s"(wg), "s"(wu), "s"(wd), "s"(out), "s"(ws));
    const int tid = threadIdx.x, lane = tid & 63, w = __builtin_amdgcn_readfirstlane(tid >> 6);
    const int xl = blockIdx.x & 7, li = blockIdx.x >> 3;
    const bool prod = li < 28;
    const int mat = li & 1, tile = 14 * xl + (li >> 1);
    const int s = 2 * xl + (li >> 4), cg = li & 15;
    const __amdgpu_buffer_rsrc_t rws = __builtin_amdgcn_make_buffer_rsrc(ws, 0, WS_BYTES, 0x00020000);

    if (w == 8) {
        if (prod) {
            const int ho = WS_H + (mat * 112 + tile) * 2048 + lane * 16;
            const v4u ones = {0xffffffffu, 0xffffffffu, 0xffffffffu, 0xffffffffu};
            __builtin_amdgcn_raw_buffer_store_b128(ones, rws, ho, 0, SC1);
            __builtin_amdgcn_raw_buffer_store_b128(ones, rws, ho + 1024, 0, SC1);
        }
        v2u ep = __builtin_amdgcn_raw_buffer_load_b64(rws, WS_EPOCH, 0, SC1);
        asm volatile("s_waitcnt vmcnt(0)" : "+v"(ep) :: "memory");
        const unsigned elo = __builtin_amdgcn_readfirstlane(ep.x), ehi = __builtin_amdgcn_readfirstlane(ep.y);
        const u64 tag = (((u64)ehi << 32) | elo) + 1u;
        const v2u tagv = {(unsigned)tag, (unsigned)(tag >> 32)};
        if (lane == 0) __builtin_amdgcn_raw_buffer_store_b64(tagv, rws, WS_FLAG + (xl * 32 + li) * 128, 0, SC1);
        STAMP(1);
        {
            const int pl = lane < 14 ? lane : 0;
            const int fo = WS_FLAG + (xl * 32 + 14 * (li >> 4) + pl) * 128;
#pragma unroll 1
            for (unsigned spin = 0; spin < SPIN_CAP; ++spin) {
                asm volatile("" ::: "memory");
                const v2u f = __builtin_amdgcn_raw_buffer_load_b64(rws, fo, 0, SC1V);
                if (__all(f.x == tagv.x && f.y == tagv.y)) break;
                __builtin_amdgcn_s_sleep(2);
            }
        }
        STAMP(2);
        if (lane == 0) *(LAS v2u*)(L + L_TAG) = tagv;
        {
            const __amdgpu_buffer_rsrc_t rd = __builtin_amdgcn_make_buffer_rsrc((void*)wd, 0, DM * DF * 2, 0x00020000);
            const int rho = lane >> 3, cpos = lane & 7;
            const int srow = ((rho & 3) << 1) | (rho >> 2);
            const int f0 = (rho >> 1) & 1;
            const int vb = (s * 224 + srow) * (DM * 2) + cg * 128;
            const int v0 = vb + ((cpos ^ (f0 << 1)) << 4), v1 = vb + ((cpos ^ ((f0 | 2) << 1)) << 4);
#pragma unroll
            for (int g = 0; g < 28; ++g)
                __builtin_amdgcn_raw_ptr_buffer_load_lds(rd, (LAS void*)(L + L_WD + g * 1024), 16, (g & 1) ? v1 : v0, g * 8 * (DM * 2), 0, DAUX);
        }
        BARRIER();
        asm volatile("s_waitcnt vmcnt(0)" ::: "memory");
        STAMP(3);
        BARRIER();
        BARRIER();
        STAMP(4);
        for (int i = 0; i < CDELAY; ++i) __builtin_amdgcn_s_sleep(1);
        const int io = WS_PART + (cg * 16 + s) * 8192 + lane * 16;
        const unsigned t8 = tagv.x & 0xffu;
        v4u p[8];
#pragma unroll 1
        for (unsigned spin = 0; spin < SPIN_CAP; ++spin) {
            asm volatile("" ::: "memory");
#pragma unroll
            for (int i = 0; i < 8; ++i) p[i] = __builtin_amdgcn_raw_buffer_load_b128(rws, io, i * 1024, SC1V);
            unsigned bad = 0;
#pragma unroll
            for (int i = 0; i < 8; ++i) bad |= (p[i].x ^ t8) | (p[i].y ^ t8) | (p[i].z ^ t8) | (p[i].w ^ t8);
            if (__all((bad & 0xffu) == 0)) break;
        }
        STAMP(5);
        f32x4 acc = {0.f, 0.f, 0.f, 0.f};
#pragma unroll
        for (int i = 0; i < 8; ++i) acc += __builtin_bit_cast(f32x4, p[i] & 0xffffff00u);
        f32x4 o;
#pragma unroll
        for (int j = 0; j < 4; ++j) o[j] = acc[j] + __shfl_xor(acc[j], 32);
        if (lane < 32) {
            const __amdgpu_buffer_rsrc_t ro = __builtin_amdgcn_make_buffer_rsrc(out, 0, 32 * DM * 2, 0x00020000);
            const v2u ov = {pack_bf16(o[0], o[1]), pack_bf16(o[2], o[3])};
            __builtin_amdgcn_raw_buffer_store_b64(ov, ro, (2 * s + (lane >> 4)) * (DM * 2) + cg * 128 + (lane & 15) * 8, 0, OAUX);
        }
        STAMP(6);
    } else {
        if (prod) {
            const __amdgpu_buffer_rsrc_t rw = __builtin_amdgcn_make_buffer_rsrc((void*)(mat ? wu : wg), 0, DM * DF * 2, 0x00020000);
            const __amdgpu_buffer_rsrc_t rx = __builtin_amdgcn_make_buffer_rsrc((void*)x, 0, 32 * DM * 2, 0x00020000);
            const int wo = (128 * w + 8 * (lane >> 4)) * (DF * 2) + tile * 64 + (lane & 15) * 4;
            const int xo = (lane & 15) * (DM * 2) + (128 * w + 8 * (lane >> 4)) * 2;
            unsigned a[32];
            v4u xf[2][4];
#pragma unroll
            for (int i = 0; i < 16; ++i) a[i] = __builtin_amdgcn_raw_buffer_load_b32(rw, wo, ((i >> 3) * 32 + (i & 7)) * (DF * 2), WAUX);
#pragma unroll
            for (int ks = 0; ks < 4; ++ks)
#pragma unroll
                for (int tt = 0; tt < 2; ++tt) xf[tt][ks] = __builtin_amdgcn_raw_buffer_load_b128(rx, xo + ks * 64, tt * 16 * (DM * 2), 0);
#pragma unroll
            for (int i = 16; i < 32; ++i) a[i] = __builtin_amdgcn_raw_buffer_load_b32(rw, wo, ((i >> 3) * 32 + (i & 7)) * (DF * 2), WAUX);
            __builtin_amdgcn_sched_barrier(0);
            STAMP(1);
            f32x4 acc[2][2] = {{{0.f, 0.f, 0.f, 0.f}, {0.f, 0.f, 0.f, 0.f}}, {{0.f, 0.f, 0.f, 0.f}, {0.f, 0.f, 0.f, 0.f}}};
#pragma unroll
            for (int ks = 0; ks < 4; ++ks) {
                v4u fe, fo;
#pragma unroll
                for (int j = 0; j < 4; ++j) {
                    fe[j] = __builtin_amdgcn_perm(a[8 * ks + 2 * j + 1], a[8 * ks + 2 * j], 0x05040100u);
                    fo[j] = __builtin_amdgcn_perm(a[8 * ks + 2 * j + 1], a[8 * ks + 2 * j], 0x07060302u);
                }
#pragma unroll
                for (int tt = 0; tt < 2; ++tt) {
                    acc[tt][0] = mfma(fe, xf[tt][ks], acc[tt][0]);
                    acc[tt][1] = mfma(fo, xf[tt][ks], acc[tt][1]);
                }
                __builtin_amdgcn_sched_barrier(0);
            }
#pragma unroll
            for (int tt = 0; tt < 2; ++tt)
#pragma unroll
                for (int par = 0; par < 2; ++par) *(LAS f32x4*)(L + L_RED + w * RED_W + tt * RED_TT + par * RED_PAR + lane * 16) = acc[tt][par];
            STAMP(2);
        }
        BARRIER();
        if (prod) {
            const int u = tid >> 2, i = tid & 3;
            const int ro = L_RED + (u >> 6) * RED_TT + (i >> 1) * RED_PAR + (u & 63) * 16 + (i & 1) * 8;
            f32x2 v = *(LAS f32x2*)(L + ro);
#pragma unroll
            for (int ww = 1; ww < 8; ++ww) v += *(LAS f32x2*)(L + ro + ww * RED_W);
            unsigned d = pack_bf16(v.x, v.y);
            if (mat == 0) d = pack_bf16(gelu_tanh(lo_f(d)), gelu_tanh(hi_f(d)));
            if (d == 0xffffffffu) d = 0x7fffffffu;
            v4u q;
            q.x = d;
            q.y = __builtin_amdgcn_update_dpp(0, (int)d, 0x55, 0xf, 0xf, true);
            q.z = __builtin_amdgcn_update_dpp(0, (int)d, 0xaa, 0xf, 0xf, true);
            q.w = __builtin_amdgcn_update_dpp(0, (int)d, 0xff, 0xf, 0xf, true);
            if (i == 0) __builtin_amdgcn_raw_buffer_store_b128(q, rws, WS_H + (mat * 112 + tile) * 2048 + u * 16, 0, SC1);
        }
        STAMP(3);
        const v2u tagv = *(LAS v2u*)(L + L_TAG);
        const int tt = w >> 2, c16 = w & 3;
        const int po = WS_PART + ((cg * 16 + 8 * tt + ((lane & 15) >> 1)) * 16 + s) * 512 + (lane & 1) * 256 + c16 * 64 + (lane >> 4) * 16;
        const int ho = WS_H + (7 * s + (w < 7 ? w : 0)) * 2048 + lane * 16;
        v4u ge[2], uu[2];
        if (w < 7) {
            ge[0] = __builtin_amdgcn_raw_buffer_load_b128(rws, ho, 0, SC1V);
            ge[1] = __builtin_amdgcn_raw_buffer_load_b128(rws, ho, 1024, SC1V);
            uu[0] = __builtin_amdgcn_raw_buffer_load_b128(rws, ho, 112 * 2048, SC1V);
            uu[1] = __builtin_amdgcn_raw_buffer_load_b128(rws, ho, 112 * 2048 + 1024, SC1V);
        } else if (blockIdx.x == 255) {
#pragma unroll 1
            for (unsigned spin = 0; spin < SPIN_CAP; ++spin) {
                asm volatile("" ::: "memory");
                bool ok = true;
#pragma unroll
                for (int i = 0; i < 4; ++i) {
                    const v2u f = __builtin_amdgcn_raw_buffer_load_b64(rws, WS_FLAG + lane * 128, i * 64 * 128, SC1V);
                    ok &= f.x == tagv.x && f.y == tagv.y;
                }
                if (__all(ok)) break;
                __builtin_amdgcn_s_sleep(4);
            }
            if (lane == 0) __builtin_amdgcn_raw_buffer_store_b64(tagv, rws, WS_EPOCH, 0, SC1);
        }
        __builtin_amdgcn_sched_barrier(0);
        BARRIER();
        v4u wf[7];
        {
            const int kg = lane >> 4, q = (lane & 15) >> 2, p = lane & 3;
            const int f = ((kg & 1) << 1) | (q >> 1);
            const int ao = L_WD + kg * 1024 + q * 128 + (((2 * c16 + (p >> 1)) ^ (f << 1)) << 4) + (p & 1) * 8;
#pragma unroll
            for (int ks = 0; ks < 7; ++ks) {
                const v2u lo = __builtin_bit_cast(v2u, __builtin_amdgcn_ds_read_tr16_b64_v4i16((LAS s16x4*)(L + ao + ks * 4096)));
                const v2u hi = __builtin_bit_cast(v2u, __builtin_amdgcn_ds_read_tr16_b64_v4i16((LAS s16x4*)(L + ao + ks * 4096 + 512)));
                wf[ks] = v4u{lo.x, lo.y, hi.x, hi.y};
            }
        }
        __builtin_amdgcn_sched_barrier(0);
        STAMP(4);
        if (w < 7) {
#pragma unroll 1
            for (unsigned spin = 0; spin < SPIN_CAP; ++spin) {
                bool ok = true;
#pragma unroll
                for (int j = 0; j < 4; ++j) ok &= ge[0][j] != 0xffffffffu && ge[1][j] != 0xffffffffu && uu[0][j] != 0xffffffffu && uu[1][j] != 0xffffffffu;
                if (__all(ok)) break;
                asm volatile("" ::: "memory");
                ge[0] = __builtin_amdgcn_raw_buffer_load_b128(rws, ho, 0, SC1V);
                ge[1] = __builtin_amdgcn_raw_buffer_load_b128(rws, ho, 1024, SC1V);
                uu[0] = __builtin_amdgcn_raw_buffer_load_b128(rws, ho, 112 * 2048, SC1V);
                uu[1] = __builtin_amdgcn_raw_buffer_load_b128(rws, ho, 112 * 2048 + 1024, SC1V);
            }
            STAMP(5);
#pragma unroll
            for (int t2 = 0; t2 < 2; ++t2) {
                v4u h;
#pragma unroll
                for (int j = 0; j < 4; ++j) h[j] = pack_bf16(lo_f(ge[t2][j]) * lo_f(uu[t2][j]), hi_f(ge[t2][j]) * hi_f(uu[t2][j]));
                *(LAS v4u*)(L + L_H + (w * 2 + t2) * 1024 + lane * 16) = h;
            }
        }
        BARRIER();
        f32x4 acc = {0.f, 0.f, 0.f, 0.f};
#pragma unroll
        for (int ks = 0; ks < 7; ++ks) acc = mfma(wf[ks], *(LAS v4u*)(L + L_H + (ks * 2 + tt) * 1024 + lane * 16), acc);
        const unsigned t8 = tagv.x & 0xffu;
        const v4u pv = (__builtin_bit_cast(v4u, acc) & 0xffffff00u) | t8;
        __builtin_amdgcn_raw_buffer_store_b128(pv, rws, po, 0, SC1);
        STAMP(6);
    }
    {
        u64 target = stamp[0] + 3000;
        if (PADK > 0 && ((w == 8) == (PADW == 1)) && stamp[PADK] != 0) target += stamp[PADK] - stamp[0];
        while (__builtin_amdgcn_s_memrealtime() < target) __builtin_amdgcn_s_sleep(1);
    }
}

__global__ void __launch_bounds__(256) probe_kernel(int dummy) {
    unsigned v = threadIdx.x;

    asm volatile("" :: "v"(v));
    const u64 t = __builtin_amdgcn_s_memrealtime() + 1000;
    while (__builtin_amdgcn_s_memrealtime() < t) __builtin_amdgcn_s_sleep(1);
}

extern "C" void kernel_launch(void* const* d_in, const int* in_sizes, int n_in, void* d_out, int out_size, void* d_ws, size_t ws_size,
                              hipStream_t stream) {
    (void)hipFuncSetAttribute((const void*)geglu_kernel, hipFuncAttributeMaxDynamicSharedMemorySize, L_BYTES);
    probe_kernel<<<256, 256, 0, stream>>>(0);
    geglu_kernel<<<256, 576, L_BYTES, stream>>>(d_in[0], d_in[1], d_in[2], d_in[3], d_out, d_ws);
}
```

```cpp
#define PADK 0
#define PADW 0
#define PV 4
#include <hip/hip_runtime.h>
#include <stdint.h>

#define WAUX 0
#define DAUX 0
#define OAUX 0
#define PREWC 0
#define CDELAY 8

#define LAS __attribute__((address_space(3)))
typedef unsigned v4u __attribute__((ext_vector_type(4)));
typedef unsigned v2u __attribute__((ext_vector_type(2)));
typedef float f32x4 __attribute__((ext_vector_type(4)));
typedef float f32x2 __attribute__((ext_vector_type(2)));
typedef short s16x8 __attribute__((ext_vector_type(8)));
typedef short s16x4 __attribute__((ext_vector_type(4)));
typedef __bf16 bf16x8 __attribute__((ext_vector_type(8)));
typedef __bf16 bf16x2 __attribute__((ext_vector_type(2)));
typedef unsigned long long u64;

constexpr int DM = 1024, DF = 3584;
constexpr int SC1 = 16;
constexpr int SC1V = 16 | (1 << 31);
constexpr int WS_EPOCH = 0, WS_FLAG = 4096, WS_H = 65536, WS_PART = 1 << 20, WS_BYTES = WS_PART + 16 * 16 * 16 * 512;
constexpr int L_RED = 0, RED_PAR = 1024 + 128, RED_TT = 2 * RED_PAR, RED_W = 2 * RED_TT;
constexpr int L_WD = 8 * RED_W;
constexpr int L_H = L_WD + 28 * 1024;
constexpr int L_TAG = L_H + 14 * 1024;
constexpr int L_BYTES = 84 * 1024;
static_assert(L_TAG + 16 <= L_BYTES, "lds");
constexpr unsigned SPIN_CAP = 1u << 16;

constexpr int WS_STAMP = 8 << 20;
#define STAMP(i) do { __builtin_amdgcn_sched_barrier(0); stamp[i] = __builtin_amdgcn_s_memrealtime(); __builtin_amdgcn_sched_barrier(0); } while (0)

__device__ __forceinline__ f32x4 mfma(v4u a, v4u b, f32x4 c) {
    return __builtin_amdgcn_mfma_f32_16x16x32_bf16(__builtin_bit_cast(bf16x8, a), __builtin_bit_cast(bf16x8, b), c, 0, 0, 0);
}
__device__ __forceinline__ unsigned pack_bf16(float lo, float hi) {
    bf16x2 r; r.x = (__bf16)lo; r.y = (__bf16)hi; return __builtin_bit_cast(unsigned, r);
}
__device__ __forceinline__ float lo_f(unsigned d) { return __builtin_bit_cast(float, d << 16); }
__device__ __forceinline__ float hi_f(unsigned d) { return __builtin_bit_cast(float, d & 0xffff0000u); }
__device__ __forceinline__ float gelu_tanh(float g) {
    const float y2 = g * (2.0f * 0.7978845608028654f * 1.4426950408889634f) * (1.0f + 0.044715f * g * g);
    return g * __builtin_amdgcn_rcpf(1.0f + __builtin_amdgcn_exp2f(-y2));
}
#define BARRIER() do { asm volatile("s_waitcnt lgkmcnt(0)" ::: "memory"); __builtin_amdgcn_s_barrier(); asm volatile("" ::: "memory"); } while (0)

__global__ void __launch_bounds__(576, 1) geglu_kernel(const void* __restrict__ x, const void* __restrict__ wg, const void* __restrict__ wu,
                                                      const void* __restrict__ wd, void* __restrict__ out, void* ws) {
    extern __shared__ __attribute__((aligned(16))) unsigned char smem[];
    LAS unsigned char* L = (LAS unsigned char*)smem;
    u64 stamp[8] = {0, 0, 0, 0, 0, 0, 0, 0};
    STAMP(0);
    asm volatile("" :: "s"(x), "s"(wg), "s"(wu), "s"(wd), "s"(out), "s"(ws));
    const int tid = threadIdx.x, lane = tid & 63, w = __builtin_amdgcn_readfirstlane(tid >> 6);
    const int xl = blockIdx.x & 7, li = blockIdx.x >> 3;
    const bool prod = li < 28;
    const int mat = li & 1, tile = 14 * xl + (li >> 1);
    const int s = 2 * xl + (li >> 4), cg = li & 15;
    const __amdgpu_buffer_rsrc_t rws = __builtin_amdgcn_make_buffer_rsrc(ws, 0, WS_BYTES, 0x00020000);

    if (w == 8) {
        if (prod) {
            const int ho = WS_H + (mat * 112 + tile) * 2048 + lane * 16;
            const v4u ones = {0xffffffffu, 0xffffffffu, 0xffffffffu, 0xffffffffu};
            __builtin_amdgcn_raw_buffer_store_b128(ones, rws, ho, 0, SC1);
            __builtin_amdgcn_raw_buffer_store_b128(ones, rws, ho + 1024, 0, SC1);
        }
        v2u ep = __builtin_amdgcn_raw_buffer_load_b64(rws, WS_EPOCH, 0, SC1);
        asm volatile("s_waitcnt vmcnt(0)" : "+v"(ep) :: "memory");
        const unsigned elo = __builtin_amdgcn_readfirstlane(ep.x), ehi = __builtin_amdgcn_readfirstlane(ep.y);
        const u64 tag = (((u64)ehi << 32) | elo) + 1u;
        const v2u tagv = {(unsigned)tag, (unsigned)(tag >> 32)};
        if (lane == 0) __builtin_amdgcn_raw_buffer_store_b64(tagv, rws, WS_FLAG + (xl * 32 + li) * 128, 0, SC1);
        STAMP(1);
        {
            const int pl = lane < 14 ? lane : 0;
            const int fo = WS_FLAG + (xl * 32 + 14 * (li >> 4) + pl) * 128;
#pragma unroll 1
            for (unsigned spin = 0; spin < SPIN_CAP; ++spin) {
                asm volatile("" ::: "memory");
                const v2u f = __builtin_amdgcn_raw_buffer_load_b64(rws, fo, 0, SC1V);
                if (__all(f.x == tagv.x && f.y == tagv.y)) break;
                __builtin_amdgcn_s_sleep(2);
            }
        }
        STAMP(2);
        if (lane == 0) *(LAS v2u*)(L + L_TAG) = tagv;
        {
            const __amdgpu_buffer_rsrc_t rd = __builtin_amdgcn_make_buffer_rsrc((void*)wd, 0, DM * DF * 2, 0x00020000);
            const int rho = lane >> 3, cpos = lane & 7;
            const int srow = ((rho & 3) << 1) | (rho >> 2);
            const int f0 = (rho >> 1) & 1;
            const int vb = (s * 224 + srow) * (DM * 2) + cg * 128;
            const int v0 = vb + ((cpos ^ (f0 << 1)) << 4), v1 = vb + ((cpos ^ ((f0 | 2) << 1)) << 4);
#pragma unroll
            for (int g = 0; g < 28; ++g)
                __builtin_amdgcn_raw_ptr_buffer_load_lds(rd, (LAS void*)(L + L_WD + g * 1024), 16, (g & 1) ? v1 : v0, g * 8 * (DM * 2), 0, DAUX);
        }
        BARRIER();
        asm volatile("s_waitcnt vmcnt(0)" ::: "memory");
        STAMP(3);
        BARRIER();
        BARRIER();
        STAMP(4);
        for (int i = 0; i < CDELAY; ++i) __builtin_amdgcn_s_sleep(1);
        const int io = WS_PART + (cg * 16 + s) * 8192 + lane * 16;
        const unsigned t8 = tagv.x & 0xffu;
        v4u p[8];
#pragma unroll 1
        for (unsigned spin = 0; spin < SPIN_CAP; ++spin) {
            asm volatile("" ::: "memory");
#pragma unroll
            for (int i = 0; i < 8; ++i) p[i] = __builtin_amdgcn_raw_buffer_load_b128(rws, io, i * 1024, SC1V);
            unsigned bad = 0;
#pragma unroll
            for (int i = 0; i < 8; ++i) bad |= (p[i].x ^ t8) | (p[i].y ^ t8) | (p[i].z ^ t8) | (p[i].w ^ t8);
            if (__all((bad & 0xffu) == 0)) break;
        }
        STAMP(5);
        f32x4 acc = {0.f, 0.f, 0.f, 0.f};
#pragma unroll
        for (int i = 0; i < 8; ++i) acc += __builtin_bit_cast(f32x4, p[i] & 0xffffff00u);
        f32x4 o;
#pragma unroll
        for (int j = 0; j < 4; ++j) o[j] = acc[j] + __shfl_xor(acc[j], 32);
        if (lane < 32) {
            const __amdgpu_buffer_rsrc_t ro = __builtin_amdgcn_make_buffer_rsrc(out, 0, 32 * DM * 2, 0x00020000);
            const v2u ov = {pack_bf16(o[0], o[1]), pack_bf16(o[2], o[3])};
            __builtin_amdgcn_raw_buffer_store_b64(ov, ro, (2 * s + (lane >> 4)) * (DM * 2) + cg * 128 + (lane & 15) * 8, 0, OAUX);
        }
        STAMP(6);
    } else {
        if (prod) {
            const __amdgpu_buffer_rsrc_t rw = __builtin_amdgcn_make_buffer_rsrc((void*)(mat ? wu : wg), 0, DM * DF * 2, 0x00020000);
            const __amdgpu_buffer_rsrc_t rx = __builtin_amdgcn_make_buffer_rsrc((void*)x, 0, 32 * DM * 2, 0x00020000);
            const int wo = (128 * w + 8 * (lane >> 4)) * (DF * 2) + tile * 64 + (lane & 15) * 4;
            const int xo = (lane & 15) * (DM * 2) + (128 * w + 8 * (lane >> 4)) * 2;
            unsigned a[32];
            v4u xf[2][4];
#pragma unroll
            for (int i = 0; i < 16; ++i) a[i] = __builtin_amdgcn_raw_buffer_load_b32(rw, wo, ((i >> 3) * 32 + (i & 7)) * (DF * 2), WAUX);
#pragma unroll
            for (int ks = 0; ks < 4; ++ks)
#pragma unroll
                for (int tt = 0; tt < 2; ++tt) xf[tt][ks] = __builtin_amdgcn_raw_buffer_load_b128(rx, xo + ks * 64, tt * 16 * (DM * 2), 0);
#pragma unroll
            for (int i = 16; i < 32; ++i) a[i] = __builtin_amdgcn_raw_buffer_load_b32(rw, wo, ((i >> 3) * 32 + (i & 7)) * (DF * 2), WAUX);
            __builtin_amdgcn_sched_barrier(0);
            STAMP(1);
            f32x4 acc[2][2] = {{{0.f, 0.f, 0.f, 0.f}, {0.f, 0.f, 0.f, 0.f}}, {{0.f, 0.f, 0.f, 0.f}, {0.f, 0.f, 0.f, 0.f}}};
#pragma unroll
            for (int ks = 0; ks < 4; ++ks) {
                v4u fe, fo;
#pragma unroll
                for (int j = 0; j < 4; ++j) {
                    fe[j] = __builtin_amdgcn_perm(a[8 * ks + 2 * j + 1], a[8 * ks + 2 * j], 0x05040100u);
                    fo[j] = __builtin_amdgcn_perm(a[8 * ks + 2 * j + 1], a[8 * ks + 2 * j], 0x07060302u);
                }
#pragma unroll
                for (int tt = 0; tt < 2; ++tt) {
                    acc[tt][0] = mfma(fe, xf[tt][ks], acc[tt][0]);
                    acc[tt][1] = mfma(fo, xf[tt][ks], acc[tt][1]);
                }
                __builtin_amdgcn_sched_barrier(0);
            }
#pragma unroll
            for (int tt = 0; tt < 2; ++tt)
#pragma unroll
                for (int par = 0; par < 2; ++par) *(LAS f32x4*)(L + L_RED + w * RED_W + tt * RED_TT + par * RED_PAR + lane * 16) = acc[tt][par];
            STAMP(2);
        }
        BARRIER();
        if (prod) {
            const int u = tid >> 2, i = tid & 3;
            const int ro = L_RED + (u >> 6) * RED_TT + (i >> 1) * RED_PAR + (u & 63) * 16 + (i & 1) * 8;
            f32x2 v = *(LAS f32x2*)(L + ro);
#pragma unroll
            for (int ww = 1; ww < 8; ++ww) v += *(LAS f32x2*)(L + ro + ww * RED_W);
            unsigned d = pack_bf16(v.x, v.y);
            if (mat == 0) d = pack_bf16(gelu_tanh(lo_f(d)), gelu_tanh(hi_f(d)));
            if (d == 0xffffffffu) d = 0x7fffffffu;
            v4u q;
            q.x = d;
            q.y = __builtin_amdgcn_update_dpp(0, (int)d, 0x55, 0xf, 0xf, true);
            q.z = __builtin_amdgcn_update_dpp(0, (int)d, 0xaa, 0xf, 0xf, true);
            q.w = __builtin_amdgcn_update_dpp(0, (int)d, 0xff, 0xf, 0xf, true);
            if (i == 0) __builtin_amdgcn_raw_buffer_store_b128(q, rws, WS_H + (mat * 112 + tile) * 2048 + u * 16, 0, SC1);
        }
        STAMP(3);
        const v2u tagv = *(LAS v2u*)(L + L_TAG);
        const int tt = w >> 2, c16 = w & 3;
        const int po = WS_PART + ((cg * 16 + 8 * tt + ((lane & 15) >> 1)) * 16 + s) * 512 + (lane & 1) * 256 + c16 * 64 + (lane >> 4) * 16;
        const int ho = WS_H + (7 * s + (w < 7 ? w : 0)) * 2048 + lane * 16;
        v4u ge[2], uu[2];
        if (w < 7) {
            ge[0] = __builtin_amdgcn_raw_buffer_load_b128(rws, ho, 0, SC1V);
            ge[1] = __builtin_amdgcn_raw_buffer_load_b128(rws, ho, 1024, SC1V);
            uu[0] = __builtin_amdgcn_raw_buffer_load_b128(rws, ho, 112 * 2048, SC1V);
            uu[1] = __builtin_amdgcn_raw_buffer_load_b128(rws, ho, 112 * 2048 + 1024, SC1V);
        } else if (blockIdx.x == 255) {
#pragma unroll 1
            for (unsigned spin = 0; spin < SPIN_CAP; ++spin) {
                asm volatile("" ::: "memory");
                bool ok = true;
#pragma unroll
                for (int i = 0; i < 4; ++i) {
                    const v2u f = __builtin_amdgcn_raw_buffer_load_b64(rws, WS_FLAG + lane * 128, i * 64 * 128, SC1V);
                    ok &= f.x == tagv.x && f.y == tagv.y;
                }
                if (__all(ok)) break;
                __builtin_amdgcn_s_sleep(4);
            }
            if (lane == 0) __builtin_amdgcn_raw_buffer_store_b64(tagv, rws, WS_EPOCH, 0, SC1);
        }
        __builtin_amdgcn_sched_barrier(0);
        BARRIER();
        v4u wf[7];
        {
            const int kg = lane >> 4, q = (lane & 15) >> 2, p = lane & 3;
            const int f = ((kg & 1) << 1) | (q >> 1);
            const int ao = L_WD + kg * 1024 + q * 128 + (((2 * c16 + (p >> 1)) ^ (f << 1)) << 4) + (p & 1) * 8;
#pragma unroll
            for (int ks = 0; ks < 7; ++ks) {
                const v2u lo = __builtin_bit_cast(v2u, __builtin_amdgcn_ds_read_tr16_b64_v4i16((LAS s16x4*)(L + ao + ks * 4096)));
                const v2u hi = __builtin_bit_cast(v2u, __builtin_amdgcn_ds_read_tr16_b64_v4i16((LAS s16x4*)(L + ao + ks * 4096 + 512)));
                wf[ks] = v4u{lo.x, lo.y, hi.x, hi.y};
            }
        }
        __builtin_amdgcn_sched_barrier(0);
        STAMP(4);
        if (w < 7) {
#pragma unroll 1
            for (unsigned spin = 0; spin < SPIN_CAP; ++spin) {
                bool ok = true;
#pragma unroll
                for (int j = 0; j < 4; ++j) ok &= ge[0][j] != 0xffffffffu && ge[1][j] != 0xffffffffu && uu[0][j] != 0xffffffffu && uu[1][j] != 0xffffffffu;
                if (__all(ok)) break;
                asm volatile("" ::: "memory");
                ge[0] = __builtin_amdgcn_raw_buffer_load_b128(rws, ho, 0, SC1V);
                ge[1] = __builtin_amdgcn_raw_buffer_load_b128(rws, ho, 1024, SC1V);
                uu[0] = __builtin_amdgcn_raw_buffer_load_b128(rws, ho, 112 * 2048, SC1V);
                uu[1] = __builtin_amdgcn_raw_buffer_load_b128(rws, ho, 112 * 2048 + 1024, SC1V);
            }
            STAMP(5);
#pragma unroll
            for (int t2 = 0; t2 < 2; ++t2) {
                v4u h;
#pragma unroll
                for (int j = 0; j < 4; ++j) h[j] = pack_bf16(lo_f(ge[t2][j]) * lo_f(uu[t2][j]), hi_f(ge[t2][j]) * hi_f(uu[t2][j]));
                *(LAS v4u*)(L + L_H + (w * 2 + t2) * 1024 + lane * 16) = h;
            }
        }
        BARRIER();
        f32x4 acc = {0.f, 0.f, 0.f, 0.f};
#pragma unroll
        for (int ks = 0; ks < 7; ++ks) acc = mfma(wf[ks], *(LAS v4u*)(L + L_H + (ks * 2 + tt) * 1024 + lane * 16), acc);
        const unsigned t8 = tagv.x & 0xffu;
        const v4u pv = (__builtin_bit_cast(v4u, acc) & 0xffffff00u) | t8;
        __builtin_amdgcn_raw_buffer_store_b128(pv, rws, po, 0, SC1);
        STAMP(6);
    }
    {
        u64 target = stamp[0] + 3000;
        if (PADK > 0 && ((w == 8) == (PADW == 1)) && stamp[PADK] != 0) target += stamp[PADK] - stamp[0];
        while (__builtin_amdgcn_s_memrealtime() < target) __builtin_amdgcn_s_sleep(1);
    }
}

#define PDMA(rs, ldsaddr, vo, so) asm volatile("s_mov_b32 m0, %0\n\ts_nop 0\n\tbuffer_load_dwordx4 %1, %2, %3 offen lds" :: "s"(ldsaddr), "v"(vo), "s"(rs), "s"(so) : "memory")
#define PWAITV(n) asm volatile("s_waitcnt vmcnt(%0)" :: "n"(n) : "memory")
#define PRIO(x) __builtin_amdgcn_s_setprio(x)
__device__ __forceinline__ v4u ptr_frag(LAS unsigned char* p) {
    const v2u lo = __builtin_bit_cast(v2u, __builtin_amdgcn_ds_read_tr16_b64_v4i16((LAS s16x4*)p));
    const v2u hi = __builtin_bit_cast(v2u, __builtin_amdgcn_ds_read_tr16_b64_v4i16((LAS s16x4*)(p + 512)));
    return v4u{lo.x, lo.y, hi.x, hi.y};
}
__global__ void __launch_bounds__(512) probe_kernel(const void* x, const void* wg, const void* wu, const void* wd) {
    extern __shared__ __attribute__((aligned(16))) unsigned char psmem[];
    LAS unsigned char* L = (LAS unsigned char*)psmem;
    const int tid = threadIdx.x, lane = tid & 63, w = __builtin_amdgcn_readfirstlane(tid >> 6);
    const int xl = blockIdx.x & 7, li = blockIdx.x >> 3;
    f32x4 acc = {0.f, 0.f, 0.f, 0.f};
    if (PV == 4) asm volatile("" :: "s"(x), "s"(wg), "s"(wu), "s"(wd));
    if (PV >= 0 && PV < 4) {
        const int role = li & 3, kh = role & 1, tile = 7 * xl + (li >> 2);
        const __amdgpu_buffer_rsrc_t rw = __builtin_amdgcn_make_buffer_rsrc((void*)((role & 2) ? wu : wg), 0, DM * DF * 2, 0x00020000);
        const __amdgpu_buffer_rsrc_t rx = __builtin_amdgcn_make_buffer_rsrc((void*)x, 0, 32 * DM * 2, 0x00020000);
        const __amdgpu_buffer_rsrc_t rd = __builtin_amdgcn_make_buffer_rsrc((void*)wd, 0, DM * DF * 2, 0x00020000);
        const int rho = lane >> 3, trq = (lane & 15) >> 2, trp = lane & 3, kg = lane >> 4, tok = lane & 15;
        const int tt = w >> 2, c16 = w & 3;
        unsigned tv = 0;
        if (PV == 3) {
            const int n = w * 32 + li, r8 = n >> 1;
            const __amdgpu_buffer_rsrc_t rp = __builtin_amdgcn_make_buffer_rsrc((void*)((n & 1) ? wu : wg), 0, DM * DF * 2, 0x00020000);
            const int lr = lane / 7, lc = lane - 7 * lr;
            const int row = ((r8 >> 4) & 1) * 512 + (r8 >> 5) * 128 + (r8 & 15) * 8 + lr;
            PRIO(3);
            asm volatile("buffer_load_dword %0, %1, %2, 0 offen" : "=v"(tv) : "v"(lane < 56 ? row * (DF * 2) + xl * 896 + lc * 128 : 0x7ff00000), "s"(rp) : "memory");
            __builtin_amdgcn_s_barrier();
        }
        if (li < 28) {
            const int wv0 = (512 * kh + 16 * w + rho) * (DF * 2) + tile * 128 + (((lane & 7) ^ (rho & 2)) << 4), wv1 = (wv0 ^ 64) + 8 * (DF * 2);
#define PWCHUNK(c) do { PDMA(rw, 32768 + (c) * 16384 + w * 2048, wv0, (c) * 128 * (DF * 2)); PDMA(rw, 32768 + (c) * 16384 + w * 2048 + 1024, wv1, (c) * 128 * (DF * 2)); } while (0)
            const int xro = (16 * tt + tok) * 1024, xsw = kg ^ tok;
            const int tro = 32768 + kg * 1024 + trq * 128 + (((2 * c16 + (trp >> 1)) ^ ((((kg & 1) << 1) | (trq >> 1)) << 1)) << 4) + (trp & 1) * 8;
            PRIO(3); PWCHUNK(0);
#pragma unroll
            for (int j = 0; j < 4; ++j) PDMA(rx, (4 * w + j) * 1024, (4 * w + j) * 2048 + ((lane ^ ((4 * w + j) & 15)) << 4), kh * 1024);
            PRIO(2); PWCHUNK(1); PRIO(1); PWCHUNK(2); PWCHUNK(3); PRIO(0);
#define PCOMPUTE(c) do { _Pragma("unroll") for (int k = 0; k < 4; ++k) acc = mfma(ptr_frag(L + tro + (c) * 16384 + k * 4096), *(LAS v4u*)(L + xro + (c) * 256 + ((xsw ^ (4 * k)) << 4)), acc); __builtin_amdgcn_sched_barrier(0); } while (0)
            if (PV == 1) { PWAITV(6); BARRIER(); PCOMPUTE(0); PWAITV(4); BARRIER(); PCOMPUTE(1); PWAITV(2); BARRIER(); PCOMPUTE(2); PWAITV(0); BARRIER(); PCOMPUTE(3); }
        }
        if (PV == 2 && w < 7) {
            const int s = 2 * xl + (li & 1), cg = li >> 1;
            const int dvo = (s * 224 + w * 32 + rho) * (DM * 2) + cg * 128 + (((lane & 7) ^ (rho & 6)) << 4);
#pragma unroll
            for (int j = 0; j < 4; ++j) PDMA(rd, 98304 + (4 * w + j) * 1024, dvo, j * 8 * (DM * 2));
        }
        asm volatile("s_waitcnt vmcnt(0)" : "+v"(tv) :: "memory");
    }
    float z = acc[0] + acc[1];
    asm volatile("" : "+v"(z));
    __builtin_amdgcn_sched_barrier(0);
    const u64 t = __builtin_amdgcn_s_memrealtime() + 1000;
    while (__builtin_amdgcn_s_memrealtime() < t) __builtin_amdgcn_s_sleep(1);
}

extern "C" void kernel_launch(void* const* d_in, const int* in_sizes, int n_in, void* d_out, int out_size, void* d_ws, size_t ws_size,
                              hipStream_t stream) {
    (void)hipFuncSetAttribute((const void*)geglu_kernel, hipFuncAttributeMaxDynamicSharedMemorySize, L_BYTES);
    (void)hipFuncSetAttribute((const void*)probe_kernel, hipFuncAttributeMaxDynamicSharedMemorySize, 140 * 1024);
    probe_kernel<<<256, 512, 140 * 1024, stream>>>(d_in[0], d_in[1], d_in[2], d_in[3]);
    geglu_kernel<<<256, 576, L_BYTES, stream>>>(d_in[0], d_in[1], d_in[2], d_in[3], d_out, d_ws);
}
```

```cpp
#define PADK 0
#define PADW 0
#define CLM 257
#include <hip/hip_runtime.h>
#include <stdint.h>

#define WAUX 0
#define DAUX 0
#define OAUX 0
#define PREWC 0
#define CDELAY 8

#define LAS __attribute__((address_space(3)))
typedef unsigned v4u __attribute__((ext_vector_type(4)));
typedef unsigned v2u __attribute__((ext_vector_type(2)));
typedef float f32x4 __attribute__((ext_vector_type(4)));
typedef float f32x2 __attribute__((ext_vector_type(2)));
typedef short s16x8 __attribute__((ext_vector_type(8)));
typedef short s16x4 __attribute__((ext_vector_type(4)));
typedef __bf16 bf16x8 __attribute__((ext_vector_type(8)));
typedef __bf16 bf16x2 __attribute__((ext_vector_type(2)));
typedef unsigned long long u64;

constexpr int DM = 1024, DF = 3584;
constexpr int SC1 = 16;
constexpr int SC1V = 16 | (1 << 31);
constexpr int WS_EPOCH = 0, WS_FLAG = 4096, WS_H = 65536, WS_PART = 1 << 20, WS_BYTES = WS_PART + 16 * 16 * 16 * 512;
constexpr int L_RED = 0, RED_PAR = 1024 + 128, RED_TT = 2 * RED_PAR, RED_W = 2 * RED_TT;
constexpr int L_WD = 8 * RED_W;
constexpr int L_H = L_WD + 28 * 1024;
constexpr int L_TAG = L_H + 14 * 1024;
constexpr int L_BYTES = 84 * 1024;
static_assert(L_TAG + 16 <= L_BYTES, "lds");
constexpr unsigned SPIN_CAP = 1u << 16;

constexpr int WS_STAMP = 8 << 20;
#define STAMP(i) do { __builtin_amdgcn_sched_barrier(0); stamp[i] = __builtin_amdgcn_s_memrealtime(); __builtin_amdgcn_sched_barrier(0); } while (0)

__device__ __forceinline__ f32x4 mfma(v4u a, v4u b, f32x4 c) {
    return __builtin_amdgcn_mfma_f32_16x16x32_bf16(__builtin_bit_cast(bf16x8, a), __builtin_bit_cast(bf16x8, b), c, 0, 0, 0);
}
__device__ __forceinline__ unsigned pack_bf16(float lo, float hi) {
    bf16x2 r; r.x = (__bf16)lo; r.y = (__bf16)hi; return __builtin_bit_cast(unsigned, r);
}
__device__ __forceinline__ float lo_f(unsigned d) { return __builtin_bit_cast(float, d << 16); }
__device__ __forceinline__ float hi_f(unsigned d) { return __builtin_bit_cast(float, d & 0xffff0000u); }
__device__ __forceinline__ float gelu_tanh(float g) {
    const float y2 = g * (2.0f * 0.7978845608028654f * 1.4426950408889634f) * (1.0f + 0.044715f * g * g);
    return g * __builtin_amdgcn_rcpf(1.0f + __builtin_amdgcn_exp2f(-y2));
}
#define BARRIER() do { asm volatile("s_waitcnt lgkmcnt(0)" ::: "memory"); __builtin_amdgcn_s_barrier(); asm volatile("" ::: "memory"); } while (0)

__global__ void __launch_bounds__(576, 1) geglu_kernel(const void* __restrict__ x, const void* __restrict__ wg, const void* __restrict__ wu,
                                                      const void* __restrict__ wd, void* __restrict__ out, void* ws) {
    extern __shared__ __attribute__((aligned(16))) unsigned char smem[];
    LAS unsigned char* L = (LAS unsigned char*)smem;
    u64 stamp[8] = {0, 0, 0, 0, 0, 0, 0, 0};
    STAMP(0);
    asm volatile("" :: "s"(x), "s"(wg), "s"(wu), "s"(wd), "s"(out), "s"(ws));
    const int tid = threadIdx.x, lane = tid & 63, w = __builtin_amdgcn_readfirstlane(tid >> 6);
    const int xl = blockIdx.x & 7, li = blockIdx.x >> 3;
    const bool prod = li < 28;
    const int mat = li & 1, tile = 14 * xl + (li >> 1);
    const int s = 2 * xl + (li >> 4), cg = li & 15;
    const __amdgpu_buffer_rsrc_t rws = __builtin_amdgcn_make_buffer_rsrc(ws, 0, WS_BYTES, 0x00020000);

    if (w == 8) {
        if (prod) {
            const int ho = WS_H + (mat * 112 + tile) * 2048 + lane * 16;
            const v4u ones = {0xffffffffu, 0xffffffffu, 0xffffffffu, 0xffffffffu};
            __builtin_amdgcn_raw_buffer_store_b128(ones, rws, ho, 0, SC1);
            __builtin_amdgcn_raw_buffer_store_b128(ones, rws, ho + 1024, 0, SC1);
        }
        v2u ep = __builtin_amdgcn_raw_buffer_load_b64(rws, WS_EPOCH, 0, SC1);
        asm volatile("s_waitcnt vmcnt(0)" : "+v"(ep) :: "memory");
        const unsigned elo = __builtin_amdgcn_readfirstlane(ep.x), ehi = __builtin_amdgcn_readfirstlane(ep.y);
        const u64 tag = (((u64)ehi << 32) | elo) + 1u;
        const v2u tagv = {(unsigned)tag, (unsigned)(tag >> 32)};
        if (lane == 0) __builtin_amdgcn_raw_buffer_store_b64(tagv, rws, WS_FLAG + (xl * 32 + li) * 128, 0, SC1);
        STAMP(1);
        {
            const int pl = lane < 14 ? lane : 0;
            const int fo = WS_FLAG + (xl * 32 + 14 * (li >> 4) + pl) * 128;
#pragma unroll 1
            for (unsigned spin = 0; spin < SPIN_CAP; ++spin) {
                asm volatile("" ::: "memory");
                const v2u f = __builtin_amdgcn_raw_buffer_load_b64(rws, fo, 0, SC1V);
                if (__all(f.x == tagv.x && f.y == tagv.y)) break;
                __builtin_amdgcn_s_sleep(2);
            }
        }
        STAMP(2);
        if (lane == 0) *(LAS v2u*)(L + L_TAG) = tagv;
        {
            const __amdgpu_buffer_rsrc_t rd = __builtin_amdgcn_make_buffer_rsrc((void*)wd, 0, DM * DF * 2, 0x00020000);
            const int rho = lane >> 3, cpos = lane & 7;
            const int srow = ((rho & 3) << 1) | (rho >> 2);
            const int f0 = (rho >> 1) & 1;
            const int vb = (s * 224 + srow) * (DM * 2) + cg * 128;
            const int v0 = vb + ((cpos ^ (f0 << 1)) << 4), v1 = vb + ((cpos ^ ((f0 | 2) << 1)) << 4);
#pragma unroll
            for (int g = 0; g < 28; ++g)
                __builtin_amdgcn_raw_ptr_buffer_load_lds(rd, (LAS void*)(L + L_WD + g * 1024), 16, (g & 1) ? v1 : v0, g * 8 * (DM * 2), 0, DAUX);
        }
        BARRIER();
        asm volatile("s_waitcnt vmcnt(0)" ::: "memory");
        STAMP(3);
        BARRIER();
        BARRIER();
        STAMP(4);
        for (int i = 0; i < CDELAY; ++i) __builtin_amdgcn_s_sleep(1);
        const int io = WS_PART + (cg * 16 + s) * 8192 + lane * 16;
        const unsigned t8 = tagv.x & 0xffu;
        v4u p[8];
#pragma unroll 1
        for (unsigned spin = 0; spin < SPIN_CAP; ++spin) {
            asm volatile("" ::: "memory");
#pragma unroll
            for (int i = 0; i < 8; ++i) p[i] = __builtin_amdgcn_raw_buffer_load_b128(rws, io, i * 1024, SC1V);
            unsigned bad = 0;
#pragma unroll
            for (int i = 0; i < 8; ++i) bad |= (p[i].x ^ t8) | (p[i].y ^ t8) | (p[i].z ^ t8) | (p[i].w ^ t8);
            if (__all((bad & 0xffu) == 0)) break;
        }
        STAMP(5);
        f32x4 acc = {0.f, 0.f, 0.f, 0.f};
#pragma unroll
        for (int i = 0; i < 8; ++i) acc += __builtin_bit_cast(f32x4, p[i] & 0xffffff00u);
        f32x4 o;
#pragma unroll
        for (int j = 0; j < 4; ++j) o[j] = acc[j] + __shfl_xor(acc[j], 32);
        if (lane < 32) {
            const __amdgpu_buffer_rsrc_t ro = __builtin_amdgcn_make_buffer_rsrc(out, 0, 32 * DM * 2, 0x00020000);
            const v2u ov = {pack_bf16(o[0], o[1]), pack_bf16(o[2], o[3])};
            __builtin_amdgcn_raw_buffer_store_b64(ov, ro, (2 * s + (lane >> 4)) * (DM * 2) + cg * 128 + (lane & 15) * 8, 0, OAUX);
        }
        STAMP(6);
    } else {
        if (prod) {
            const __amdgpu_buffer_rsrc_t rw = __builtin_amdgcn_make_buffer_rsrc((void*)(mat ? wu : wg), 0, DM * DF * 2, 0x00020000);
            const __amdgpu_buffer_rsrc_t rx = __builtin_amdgcn_make_buffer_rsrc((void*)x, 0, 32 * DM * 2, 0x00020000);
            const int wo = (128 * w + 8 * (lane >> 4)) * (DF * 2) + tile * 64 + (lane & 15) * 4;
            const int xo = (lane & 15) * (DM * 2) + (128 * w + 8 * (lane >> 4)) * 2;
            unsigned a[32];
            v4u xf[2][4];
#pragma unroll
            for (int i = 0; i < 16; ++i) a[i] = __builtin_amdgcn_raw_buffer_load_b32(rw, wo, ((i >> 3) * 32 + (i & 7)) * (DF * 2), WAUX);
#pragma unroll
            for (int ks = 0; ks < 4; ++ks)
#pragma unroll
                for (int tt = 0; tt < 2; ++tt) xf[tt][ks] = __builtin_amdgcn_raw_buffer_load_b128(rx, xo + ks * 64, tt * 16 * (DM * 2), 0);
#pragma unroll
            for (int i = 16; i < 32; ++i) a[i] = __builtin_amdgcn_raw_buffer_load_b32(rw, wo, ((i >> 3) * 32 + (i & 7)) * (DF * 2), WAUX);
            __builtin_amdgcn_sched_barrier(0);
            STAMP(1);
            f32x4 acc[2][2] = {{{0.f, 0.f, 0.f, 0.f}, {0.f, 0.f, 0.f, 0.f}}, {{0.f, 0.f, 0.f, 0.f}, {0.f, 0.f, 0.f, 0.f}}};
#pragma unroll
            for (int ks = 0; ks < 4; ++ks) {
                v4u fe, fo;
#pragma unroll
                for (int j = 0; j < 4; ++j) {
                    fe[j] = __builtin_amdgcn_perm(a[8 * ks + 2 * j + 1], a[8 * ks + 2 * j], 0x05040100u);
                    fo[j] = __builtin_amdgcn_perm(a[8 * ks + 2 * j + 1], a[8 * ks + 2 * j], 0x07060302u);
                }
#pragma unroll
                for (int tt = 0; tt < 2; ++tt) {
                    acc[tt][0] = mfma(fe, xf[tt][ks], acc[tt][0]);
                    acc[tt][1] = mfma(fo, xf[tt][ks], acc[tt][1]);
                }
                __builtin_amdgcn_sched_barrier(0);
            }
#pragma unroll
            for (int tt = 0; tt < 2; ++tt)
#pragma unroll
                for (int par = 0; par < 2; ++par) *(LAS f32x4*)(L + L_RED + w * RED_W + tt * RED_TT + par * RED_PAR + lane * 16) = acc[tt][par];
            STAMP(2);
        }
        BARRIER();
        if (prod) {
            const int u = tid >> 2, i = tid & 3;
            const int ro = L_RED + (u >> 6) * RED_TT + (i >> 1) * RED_PAR + (u & 63) * 16 + (i & 1) * 8;
            f32x2 v = *(LAS f32x2*)(L + ro);
#pragma unroll
            for (int ww = 1; ww < 8; ++ww) v += *(LAS f32x2*)(L + ro + ww * RED_W);
            unsigned d = pack_bf16(v.x, v.y);
            if (mat == 0) d = pack_bf16(gelu_tanh(lo_f(d)), gelu_tanh(hi_f(d)));
            if (d == 0xffffffffu) d = 0x7fffffffu;
            v4u q;
            q.x = d;
            q.y = __builtin_amdgcn_update_dpp(0, (int)d, 0x55, 0xf, 0xf, true);
            q.z = __builtin_amdgcn_update_dpp(0, (int)d, 0xaa, 0xf, 0xf, true);
            q.w = __builtin_amdgcn_update_dpp(0, (int)d, 0xff, 0xf, 0xf, true);
            if (i == 0) __builtin_amdgcn_raw_buffer_store_b128(q, rws, WS_H + (mat * 112 + tile) * 2048 + u * 16, 0, SC1);
        }
        STAMP(3);
        const v2u tagv = *(LAS v2u*)(L + L_TAG);
        const int tt = w >> 2, c16 = w & 3;
        const int po = WS_PART + ((cg * 16 + 8 * tt + ((lane & 15) >> 1)) * 16 + s) * 512 + (lane & 1) * 256 + c16 * 64 + (lane >> 4) * 16;
        const int ho = WS_H + (7 * s + (w < 7 ? w : 0)) * 2048 + lane * 16;
        v4u ge[2], uu[2];
        if (w < 7) {
            ge[0] = __builtin_amdgcn_raw_buffer_load_b128(rws, ho, 0, SC1V);
            ge[1] = __builtin_amdgcn_raw_buffer_load_b128(rws, ho, 1024, SC1V);
            uu[0] = __builtin_amdgcn_raw_buffer_load_b128(rws, ho, 112 * 2048, SC1V);
            uu[1] = __builtin_amdgcn_raw_buffer_load_b128(rws, ho, 112 * 2048 + 1024, SC1V);
        } else if (blockIdx.x == 255) {
#pragma unroll 1
            for (unsigned spin = 0; spin < SPIN_CAP; ++spin) {
                asm volatile("" ::: "memory");
                bool ok = true;
#pragma unroll
                for (int i = 0; i < 4; ++i) {
                    const v2u f = __builtin_amdgcn_raw_buffer_load_b64(rws, WS_FLAG + lane * 128, i * 64 * 128, SC1V);
                    ok &= f.x == tagv.x && f.y == tagv.y;
                }
                if (__all(ok)) break;
                __builtin_amdgcn_s_sleep(4);
            }
            if (lane == 0) __builtin_amdgcn_raw_buffer_store_b64(tagv, rws, WS_EPOCH, 0, SC1);
        }
        __builtin_amdgcn_sched_barrier(0);
        BARRIER();
        v4u wf[7];
        {
            const int kg = lane >> 4, q = (lane & 15) >> 2, p = lane & 3;
            const int f = ((kg & 1) << 1) | (q >> 1);
            const int ao = L_WD + kg * 1024 + q * 128 + (((2 * c16 + (p >> 1)) ^ (f << 1)) << 4) + (p & 1) * 8;
#pragma unroll
            for (int ks = 0; ks < 7; ++ks) {
                const v2u lo = __builtin_bit_cast(v2u, __builtin_amdgcn_ds_read_tr16_b64_v4i16((LAS s16x4*)(L + ao + ks * 4096)));
                const v2u hi = __builtin_bit_cast(v2u, __builtin_amdgcn_ds_read_tr16_b64_v4i16((LAS s16x4*)(L + ao + ks * 4096 + 512)));
                wf[ks] = v4u{lo.x, lo.y, hi.x, hi.y};
            }
        }
        __builtin_amdgcn_sched_barrier(0);
        STAMP(4);
        if (w < 7) {
#pragma unroll 1
            for (unsigned spin = 0; spin < SPIN_CAP; ++spin) {
                bool ok = true;
#pragma unroll
                for (int j = 0; j < 4; ++j) ok &= ge[0][j] != 0xffffffffu && ge[1][j] != 0xffffffffu && uu[0][j] != 0xffffffffu && uu[1][j] != 0xffffffffu;
                if (__all(ok)) break;
                asm volatile("" ::: "memory");
                ge[0] = __builtin_amdgcn_raw_buffer_load_b128(rws, ho, 0, SC1V);
                ge[1] = __builtin_amdgcn_raw_buffer_load_b128(rws, ho, 1024, SC1V);
                uu[0] = __builtin_amdgcn_raw_buffer_load_b128(rws, ho, 112 * 2048, SC1V);
                uu[1] = __builtin_amdgcn_raw_buffer_load_b128(rws, ho, 112 * 2048 + 1024, SC1V);
            }
            STAMP(5);
#pragma unroll
            for (int t2 = 0; t2 < 2; ++t2) {
                v4u h;
#pragma unroll
                for (int j = 0; j < 4; ++j) h[j] = pack_bf16(lo_f(ge[t2][j]) * lo_f(uu[t2][j]), hi_f(ge[t2][j]) * hi_f(uu[t2][j]));
                *(LAS v4u*)(L + L_H + (w * 2 + t2) * 1024 + lane * 16) = h;
            }
        }
        BARRIER();
        f32x4 acc = {0.f, 0.f, 0.f, 0.f};
#pragma unroll
        for (int ks = 0; ks < 7; ++ks) acc = mfma(wf[ks], *(LAS v4u*)(L + L_H + (ks * 2 + tt) * 1024 + lane * 16), acc);
        const unsigned t8 = tagv.x & 0xffu;
        const v4u pv = (__builtin_bit_cast(v4u, acc) & 0xffffff00u) | t8;
        __builtin_amdgcn_raw_buffer_store_b128(pv, rws, po, 0, SC1);
        STAMP(6);
    }
    {
        u64 target = stamp[0] + 3000;
        if (PADK > 0 && ((w == 8) == (PADW == 1)) && stamp[PADK] != 0) target += stamp[PADK] - stamp[0];
        while (__builtin_amdgcn_s_memrealtime() < target) __builtin_amdgcn_s_sleep(1);
    }
}

__global__ void __launch_bounds__(512) probe_kernel(const void* wg, const void* wu, const void* wd) {
    const int tid = threadIdx.x, lane = tid & 63, w = __builtin_amdgcn_readfirstlane(tid >> 6);
    const int xl = blockIdx.x & 7, li = blockIdx.x >> 3;
    const char* base = (const char*)((li >> 3) == 0 ? wg : (li >> 3) == 1 ? wu : wd);
    unsigned acc = 0;
    if (li < 24) {
        unsigned a[2];
#pragma unroll
        for (int i = 0; i < 2; ++i) { const int j = i * 8 + w; a[i] = 0; if (j < 14) a[i] = *(const unsigned*)(base + ((size_t)xl * 7168 + (size_t)(j * 64 + lane) * 8 + (li & 7)) * 128); }
        acc = a[0] ^ a[1];
        asm volatile("" : "+v"(acc)); __builtin_amdgcn_sched_barrier(0);
        if ((CLM >> li) & 1) {
            const u64 t = __builtin_amdgcn_s_memrealtime() + 1000;
            while (__builtin_amdgcn_s_memrealtime() < t) __builtin_amdgcn_s_sleep(1);
        }
    }
}

extern "C" void kernel_launch(void* const* d_in, const int* in_sizes, int n_in, void* d_out, int out_size, void* d_ws, size_t ws_size,
                              hipStream_t stream) {
    (void)hipFuncSetAttribute((const void*)geglu_kernel, hipFuncAttributeMaxDynamicSharedMemorySize, L_BYTES);
    (void)hipFuncSetAttribute((const void*)probe_kernel, hipFuncAttributeMaxDynamicSharedMemorySize, 100 * 1024);
    probe_kernel<<<256, 512, 100 * 1024, stream>>>(d_in[1], d_in[2], d_in[3]);
    geglu_kernel<<<256, 576, L_BYTES, stream>>>(d_in[0], d_in[1], d_in[2], d_in[3], d_out, d_ws);
}
```

```cpp
#define PADK 0
#define PADW 0
#define LS 66560
#define BS 256
#define PRES 66560
#define NB 8
#include <hip/hip_runtime.h>
#include <stdint.h>

#define WAUX 0
#define DAUX 0
#define OAUX 0
#define PREWC 0
#define CDELAY 8

#define LAS __attribute__((address_space(3)))
typedef unsigned v4u __attribute__((ext_vector_type(4)));
typedef unsigned v2u __attribute__((ext_vector_type(2)));
typedef float f32x4 __attribute__((ext_vector_type(4)));
typedef float f32x2 __attribute__((ext_vector_type(2)));
typedef short s16x8 __attribute__((ext_vector_type(8)));
typedef short s16x4 __attribute__((ext_vector_type(4)));
typedef __bf16 bf16x8 __attribute__((ext_vector_type(8)));
typedef __bf16 bf16x2 __attribute__((ext_vector_type(2)));
typedef unsigned long long u64;

constexpr int DM = 1024, DF = 3584;
constexpr int SC1 = 16;
constexpr int SC1V = 16 | (1 << 31);
constexpr int WS_EPOCH = 0, WS_FLAG = 4096, WS_H = 65536, WS_PART = 1 << 20, WS_BYTES = WS_PART + 16 * 16 * 16 * 512;
constexpr int L_RED = 0, RED_PAR = 1024 + 128, RED_TT = 2 * RED_PAR, RED_W = 2 * RED_TT;
constexpr int L_WD = 8 * RED_W;
constexpr int L_H = L_WD + 28 * 1024;
constexpr int L_TAG = L_H + 14 * 1024;
constexpr int L_BYTES = 84 * 1024;
static_assert(L_TAG + 16 <= L_BYTES, "lds");
constexpr unsigned SPIN_CAP = 1u << 16;

constexpr int WS_STAMP = 8 << 20;
#define STAMP(i) do { __builtin_amdgcn_sched_barrier(0); stamp[i] = __builtin_amdgcn_s_memrealtime(); __builtin_amdgcn_sched_barrier(0); } while (0)

__device__ __forceinline__ f32x4 mfma(v4u a, v4u b, f32x4 c) {
    return __builtin_amdgcn_mfma_f32_16x16x32_bf16(__builtin_bit_cast(bf16x8, a), __builtin_bit_cast(bf16x8, b), c, 0, 0, 0);
}
__device__ __forceinline__ unsigned pack_bf16(float lo, float hi) {
    bf16x2 r; r.x = (__bf16)lo; r.y = (__bf16)hi; return __builtin_bit_cast(unsigned, r);
}
__device__ __forceinline__ float lo_f(unsigned d) { return __builtin_bit_cast(float, d << 16); }
__device__ __forceinline__ float hi_f(unsigned d) { return __builtin_bit_cast(float, d & 0xffff0000u); }
__device__ __forceinline__ float gelu_tanh(float g) {
    const float y2 = g * (2.0f * 0.7978845608028654f * 1.4426950408889634f) * (1.0f + 0.044715f * g * g);
    return g * __builtin_amdgcn_rcpf(1.0f + __builtin_amdgcn_exp2f(-y2));
}
#define BARRIER() do { asm volatile("s_waitcnt lgkmcnt(0)" ::: "memory"); __builtin_amdgcn_s_barrier(); asm volatile("" ::: "memory"); } while (0)

__global__ void __launch_bounds__(576, 1) geglu_kernel(const void* __restrict__ x, const void* __restrict__ wg, const void* __restrict__ wu,
                                                      const void* __restrict__ wd, void* __restrict__ out, void* ws) {
    extern __shared__ __attribute__((aligned(16))) unsigned char smem[];
    LAS unsigned char* L = (LAS unsigned char*)smem;
    u64 stamp[8] = {0, 0, 0, 0, 0, 0, 0, 0};
    STAMP(0);
    asm volatile("" :: "s"(x), "s"(wg), "s"(wu), "s"(wd), "s"(out), "s"(ws));
    const int tid = threadIdx.x, lane = tid & 63, w = __builtin_amdgcn_readfirstlane(tid >> 6);
    const int xl = blockIdx.x & 7, li = blockIdx.x >> 3;
    const bool prod = li < 28;
    const int mat = li & 1, tile = 14 * xl + (li >> 1);
    const int s = 2 * xl + (li >> 4), cg = li & 15;
    const __amdgpu_buffer_rsrc_t rws = __builtin_amdgcn_make_buffer_rsrc(ws, 0, WS_BYTES, 0x00020000);

    if (w == 8) {
        if (prod) {
            const int ho = WS_H + (mat * 112 + tile) * 2048 + lane * 16;
            const v4u ones = {0xffffffffu, 0xffffffffu, 0xffffffffu, 0xffffffffu};
            __builtin_amdgcn_raw_buffer_store_b128(ones, rws, ho, 0, SC1);
            __builtin_amdgcn_raw_buffer_store_b128(ones, rws, ho + 1024, 0, SC1);
        }
        v2u ep = __builtin_amdgcn_raw_buffer_load_b64(rws, WS_EPOCH, 0, SC1);
        asm volatile("s_waitcnt vmcnt(0)" : "+v"(ep) :: "memory");
        const unsigned elo = __builtin_amdgcn_readfirstlane(ep.x), ehi = __builtin_amdgcn_readfirstlane(ep.y);
        const u64 tag = (((u64)ehi << 32) | elo) + 1u;
        const v2u tagv = {(unsigned)tag, (unsigned)(tag >> 32)};
        if (lane == 0) __builtin_amdgcn_raw_buffer_store_b64(tagv, rws, WS_FLAG + (xl * 32 + li) * 128, 0, SC1);
        STAMP(1);
        {
            const int pl = lane < 14 ? lane : 0;
            const int fo = WS_FLAG + (xl * 32 + 14 * (li >> 4) + pl) * 128;
#pragma unroll 1
            for (unsigned spin = 0; spin < SPIN_CAP; ++spin) {
                asm volatile("" ::: "memory");
                const v2u f = __builtin_amdgcn_raw_buffer_load_b64(rws, fo, 0, SC1V);
                if (__all(f.x == tagv.x && f.y == tagv.y)) break;
                __builtin_amdgcn_s_sleep(2);
            }
        }
        STAMP(2);
        if (lane == 0) *(LAS v2u*)(L + L_TAG) = tagv;
        {
            const __amdgpu_buffer_rsrc_t rd = __builtin_amdgcn_make_buffer_rsrc((void*)wd, 0, DM * DF * 2, 0x00020000);
            const int rho = lane >> 3, cpos = lane & 7;
            const int srow = ((rho & 3) << 1) | (rho >> 2);
            const int f0 = (rho >> 1) & 1;
            const int vb = (s * 224 + srow) * (DM * 2) + cg * 128;
            const int v0 = vb + ((cpos ^ (f0 << 1)) << 4), v1 = vb + ((cpos ^ ((f0 | 2) << 1)) << 4);
#pragma unroll
            for (int g = 0; g < 28; ++g)
                __builtin_amdgcn_raw_ptr_buffer_load_lds(rd, (LAS void*)(L + L_WD + g * 1024), 16, (g & 1) ? v1 : v0, g * 8 * (DM * 2), 0, DAUX);
        }
        BARRIER();
        asm volatile("s_waitcnt vmcnt(0)" ::: "memory");
        STAMP(3);
        BARRIER();
        BARRIER();
        STAMP(4);
        for (int i = 0; i < CDELAY; ++i) __builtin_amdgcn_s_sleep(1);
        const int io = WS_PART + (cg * 16 + s) * 8192 + lane * 16;
        const unsigned t8 = tagv.x & 0xffu;
        v4u p[8];
#pragma unroll 1
        for (unsigned spin = 0; spin < SPIN_CAP; ++spin) {
            asm volatile("" ::: "memory");
#pragma unroll
            for (int i = 0; i < 8; ++i) p[i] = __builtin_amdgcn_raw_buffer_load_b128(rws, io, i * 1024, SC1V);
            unsigned bad = 0;
#pragma unroll
            for (int i = 0; i < 8; ++i) bad |= (p[i].x ^ t8) | (p[i].y ^ t8) | (p[i].z ^ t8) | (p[i].w ^ t8);
            if (__all((bad & 0xffu) == 0)) break;
        }
        STAMP(5);
        f32x4 acc = {0.f, 0.f, 0.f, 0.f};
#pragma unroll
        for (int i = 0; i < 8; ++i) acc += __builtin_bit_cast(f32x4, p[i] & 0xffffff00u);
        f32x4 o;
#pragma unroll
        for (int j = 0; j < 4; ++j) o[j] = acc[j] + __shfl_xor(acc[j], 32);
        if (lane < 32) {
            const __amdgpu_buffer_rsrc_t ro = __builtin_amdgcn_make_buffer_rsrc(out, 0, 32 * DM * 2, 0x00020000);
            const v2u ov = {pack_bf16(o[0], o[1]), pack_bf16(o[2], o[3])};
            __builtin_amdgcn_raw_buffer_store_b64(ov, ro, (2 * s + (lane >> 4)) * (DM * 2) + cg * 128 + (lane & 15) * 8, 0, OAUX);
        }
        STAMP(6);
    } else {
        if (prod) {
            const __amdgpu_buffer_rsrc_t rw = __builtin_amdgcn_make_buffer_rsrc((void*)(mat ? wu : wg), 0, DM * DF * 2, 0x00020000);
            const __amdgpu_buffer_rsrc_t rx = __builtin_amdgcn_make_buffer_rsrc((void*)x, 0, 32 * DM * 2, 0x00020000);
            const int wo = (128 * w + 8 * (lane >> 4)) * (DF * 2) + tile * 64 + (lane & 15) * 4;
            const int xo = (lane & 15) * (DM * 2) + (128 * w + 8 * (lane >> 4)) * 2;
            unsigned a[32];
            v4u xf[2][4];
#pragma unroll
            for (int i = 0; i < 16; ++i) a[i] = __builtin_amdgcn_raw_buffer_load_b32(rw, wo, ((i >> 3) * 32 + (i & 7)) * (DF * 2), WAUX);
#pragma unroll
            for (int ks = 0; ks < 4; ++ks)
#pragma unroll
                for (int tt = 0; tt < 2; ++tt) xf[tt][ks] = __builtin_amdgcn_raw_buffer_load_b128(rx, xo + ks * 64, tt * 16 * (DM * 2), 0);
#pragma unroll
            for (int i = 16; i < 32; ++i) a[i] = __builtin_amdgcn_raw_buffer_load_b32(rw, wo, ((i >> 3) * 32 + (i & 7)) * (DF * 2), WAUX);
            __builtin_amdgcn_sched_barrier(0);
            STAMP(1);
            f32x4 acc[2][2] = {{{0.f, 0.f, 0.f, 0.f}, {0.f, 0.f, 0.f, 0.f}}, {{0.f, 0.f, 0.f, 0.f}, {0.f, 0.f, 0.f, 0.f}}};
#pragma unroll
            for (int ks = 0; ks < 4; ++ks) {
                v4u fe, fo;
#pragma unroll
                for (int j = 0; j < 4; ++j) {
                    fe[j] = __builtin_amdgcn_perm(a[8 * ks + 2 * j + 1], a[8 * ks + 2 * j], 0x05040100u);
                    fo[j] = __builtin_amdgcn_perm(a[8 * ks + 2 * j + 1], a[8 * ks + 2 * j], 0x07060302u);
                }
#pragma unroll
                for (int tt = 0; tt < 2; ++tt) {
                    acc[tt][0] = mfma(fe, xf[tt][ks], acc[tt][0]);
                    acc[tt][1] = mfma(fo, xf[tt][ks], acc[tt][1]);
                }
                __builtin_amdgcn_sched_barrier(0);
            }
#pragma unroll
            for (int tt = 0; tt < 2; ++tt)
#pragma unroll
                for (int par = 0; par < 2; ++par) *(LAS f32x4*)(L + L_RED + w * RED_W + tt * RED_TT + par * RED_PAR + lane * 16) = acc[tt][par];
            STAMP(2);
        }
        BARRIER();
        if (prod) {
            const int u = tid >> 2, i = tid & 3;
            const int ro = L_RED + (u >> 6) * RED_TT + (i >> 1) * RED_PAR + (u & 63) * 16 + (i & 1) * 8;
            f32x2 v = *(LAS f32x2*)(L + ro);
#pragma unroll
            for (int ww = 1; ww < 8; ++ww) v += *(LAS f32x2*)(L + ro + ww * RED_W);
            unsigned d = pack_bf16(v.x, v.y);
            if (mat == 0) d = pack_bf16(gelu_tanh(lo_f(d)), gelu_tanh(hi_f(d)));
            if (d == 0xffffffffu) d = 0x7fffffffu;
            v4u q;
            q.x = d;
            q.y = __builtin_amdgcn_update_dpp(0, (int)d, 0x55, 0xf, 0xf, true);
            q.z = __builtin_amdgcn_update_dpp(0, (int)d, 0xaa, 0xf, 0xf, true);
            q.w = __builtin_amdgcn_update_dpp(0, (int)d, 0xff, 0xf, 0xf, true);
            if (i == 0) __builtin_amdgcn_raw_buffer_store_b128(q, rws, WS_H + (mat * 112 + tile) * 2048 + u * 16, 0, SC1);
        }
        STAMP(3);
        const v2u tagv = *(LAS v2u*)(L + L_TAG);
        const int tt = w >> 2, c16 = w & 3;
        const int po = WS_PART + ((cg * 16 + 8 * tt + ((lane & 15) >> 1)) * 16 + s) * 512 + (lane & 1) * 256 + c16 * 64 + (lane >> 4) * 16;
        const int ho = WS_H + (7 * s + (w < 7 ? w : 0)) * 2048 + lane * 16;
        v4u ge[2], uu[2];
        if (w < 7) {
            ge[0] = __builtin_amdgcn_raw_buffer_load_b128(rws, ho, 0, SC1V);
            ge[1] = __builtin_amdgcn_raw_buffer_load_b128(rws, ho, 1024, SC1V);
            uu[0] = __builtin_amdgcn_raw_buffer_load_b128(rws, ho, 112 * 2048, SC1V);
            uu[1] = __builtin_amdgcn_raw_buffer_load_b128(rws, ho, 112 * 2048 + 1024, SC1V);
        } else if (blockIdx.x == 255) {
#pragma unroll 1
            for (unsigned spin = 0; spin < SPIN_CAP; ++spin) {
                asm volatile("" ::: "memory");
                bool ok = true;
#pragma unroll
                for (int i = 0; i < 4; ++i) {
                    const v2u f = __builtin_amdgcn_raw_buffer_load_b64(rws, WS_FLAG + lane * 128, i * 64 * 128, SC1V);
                    ok &= f.x == tagv.x && f.y == tagv.y;
                }
                if (__all(ok)) break;
                __builtin_amdgcn_s_sleep(4);
            }
            if (lane == 0) __builtin_amdgcn_raw_buffer_store_b64(tagv, rws, WS_EPOCH, 0, SC1);
        }
        __builtin_amdgcn_sched_barrier(0);
        BARRIER();
        v4u wf[7];
        {
            const int kg = lane >> 4, q = (lane & 15) >> 2, p = lane & 3;
            const int f = ((kg & 1) << 1) | (q >> 1);
            const int ao = L_WD + kg * 1024 + q * 128 + (((2 * c16 + (p >> 1)) ^ (f << 1)) << 4) + (p & 1) * 8;
#pragma unroll
            for (int ks = 0; ks < 7; ++ks) {
                const v2u lo = __builtin_bit_cast(v2u, __builtin_amdgcn_ds_read_tr16_b64_v4i16((LAS s16x4*)(L + ao + ks * 4096)));
                const v2u hi = __builtin_bit_cast(v2u, __builtin_amdgcn_ds_read_tr16_b64_v4i16((LAS s16x4*)(L + ao + ks * 4096 + 512)));
                wf[ks] = v4u{lo.x, lo.y, hi.x, hi.y};
            }
        }
        __builtin_amdgcn_sched_barrier(0);
        STAMP(4);
        if (w < 7) {
#pragma unroll 1
            for (unsigned spin = 0; spin < SPIN_CAP; ++spin) {
                bool ok = true;
#pragma unroll
                for (int j = 0; j < 4; ++j) ok &= ge[0][j] != 0xffffffffu && ge[1][j] != 0xffffffffu && uu[0][j] != 0xffffffffu && uu[1][j] != 0xffffffffu;
                if (__all(ok)) break;
                asm volatile("" ::: "memory");
                ge[0] = __builtin_amdgcn_raw_buffer_load_b128(rws, ho, 0, SC1V);
                ge[1] = __builtin_amdgcn_raw_buffer_load_b128(rws, ho, 1024, SC1V);
                uu[0] = __builtin_amdgcn_raw_buffer_load_b128(rws, ho, 112 * 2048, SC1V);
                uu[1] = __builtin_amdgcn_raw_buffer_load_b128(rws, ho, 112 * 2048 + 1024, SC1V);
            }
            STAMP(5);
#pragma unroll
            for (int t2 = 0; t2 < 2; ++t2) {
                v4u h;
#pragma unroll
                for (int j = 0; j < 4; ++j) h[j] = pack_bf16(lo_f(ge[t2][j]) * lo_f(uu[t2][j]), hi_f(ge[t2][j]) * hi_f(uu[t2][j]));
                *(LAS v4u*)(L + L_H + (w * 2 + t2) * 1024 + lane * 16) = h;
            }
        }
        BARRIER();
        f32x4 acc = {0.f, 0.f, 0.f, 0.f};
#pragma unroll
        for (int ks = 0; ks < 7; ++ks) acc = mfma(wf[ks], *(LAS v4u*)(L + L_H + (ks * 2 + tt) * 1024 + lane * 16), acc);
        const unsigned t8 = tagv.x & 0xffu;
        const v4u pv = (__builtin_bit_cast(v4u, acc) & 0xffffff00u) | t8;
        __builtin_amdgcn_raw_buffer_store_b128(pv, rws, po, 0, SC1);
        STAMP(6);
    }
    {
        u64 target = stamp[0] + 3000;
        if (PADK > 0 && ((w == 8) == (PADW == 1)) && stamp[PADK] != 0) target += stamp[PADK] - stamp[0];
        while (__builtin_amdgcn_s_memrealtime() < target) __builtin_amdgcn_s_sleep(1);
    }
}

__global__ void __launch_bounds__(64) probe_kernel(const void* wg, const void* wu, const void* wd) {
    const u64 t0 = __builtin_amdgcn_s_memrealtime();
    const int lane = threadIdx.x, b = blockIdx.x;
    const __amdgpu_buffer_rsrc_t r = __builtin_amdgcn_make_buffer_rsrc((void*)wg, 0, 7340032, 0x00020000);
    unsigned a = 0;
    u64 t1 = t0;
    if (PRES > 0) { a = __builtin_amdgcn_raw_buffer_load_b32(r, (lane * PRES + (b * BS) % PRES + 128 * 5) % 7340032, 0, 0); asm volatile("" : "+v"(a)); __builtin_amdgcn_sched_barrier(0); t1 = __builtin_amdgcn_s_memrealtime(); __builtin_amdgcn_sched_barrier(0); }
    unsigned c = __builtin_amdgcn_raw_buffer_load_b32(r, (lane * LS + b * BS) % 7340032, 0, 0);
    asm volatile("" : "+v"(c)); __builtin_amdgcn_sched_barrier(0);
    const u64 t2 = __builtin_amdgcn_s_memrealtime();
    const u64 t = t0 + 1000 + 10 * (t2 - t1);
    while (__builtin_amdgcn_s_memrealtime() < t) __builtin_amdgcn_s_sleep(4);
}

extern "C" void kernel_launch(void* const* d_in, const int* in_sizes, int n_in, void* d_out, int out_size, void* d_ws, size_t ws_size,
                              hipStream_t stream) {
    (void)hipFuncSetAttribute((const void*)geglu_kernel, hipFuncAttributeMaxDynamicSharedMemorySize, L_BYTES);
    probe_kernel<<<NB, 64, 0, stream>>>(d_in[1], d_in[2], d_in[3]);
    geglu_kernel<<<256, 576, L_BYTES, stream>>>(d_in[0], d_in[1], d_in[2], d_in[3], d_out, d_ws);
}
```

```cpp
#include <hip/hip_runtime.h>

constexpr int LDS_BYTES = 160 * 1024;

__global__ void __launch_bounds__(1024, 4) geglu_kernel(const void* x, const void* wg, const void* wu, const void* wd, void* out, void* ws) {
    asm volatile(R"ASM(
    .set WS_FLAG, 4096                   ; 256 flag lines
    .set WS_H, 65536                     ; [matrix][k-step of 32][token half][k group][token][8 bf16]
    .set H_MAT, 229376
    .set WS_PC, 1048576                  ; [column group][tile][K slice][lane][4 f32]
    .set L_W, 65536                      ; worker LDS: x image (later steps 6, 7) | 8 x steps 0..3 | steps 4, 5
    .set L_REG, 8192
    .set L_F, 131072
    .set RED, 1024
    .set L_H, 57344                      ; down-projection LDS: w_down image | h | tag
    .set L_TAG, 73728
    .set OOB, 0x7ff00000                 ; offset past every buffer: the range check drops the access
    .set PQ, 16                          ; s_sleep units per four prefetch slots
    .set NOW, 1
    .set PB, 1
    .set FIRST, 8
    .set CQ, 12
    .set CPFN, NOW*18+17
    .set WDS, 14
    .set FDELAY, 24
    .set HSLEEP, 1
    .set CDELAY, 16
    .set SPW, 8
    .set WT, 4
    ; s[4:7] x, s[8:11] Wg or Wu, s[12:15] Wd, s[16:19] ws, s[20:23] out; s24 wave, s25 group, s26 index in the group, v1 lane
.Lg_start:
    s_load_dwordx4 s[52:55], s[0:1], 8
    s_load_dwordx2 s[4:5], s[0:1], 0
    v_readfirstlane_b32 s24, v0
    v_and_b32 v1, 63, v0
    s_lshl_b32 s10, 7, 20
    s_lshl_b32 s11, 1, 17
    s_lshr_b32 s24, s24, 6
    s_and_b32 s25, s2, 7
    s_lshr_b32 s26, s2, 3
    s_sub_u32 s27, s26, 14               ; c
    s_cbranch_scc1 .Lg_work
    ; prefetch unit of this wave: 8 rows x 7 lines of the group's column stripe (one dword per line, value unused); rows past the end are dropped
    s_mul_i32 s28, s24, 18
    s_add_u32 s28, s28, s27              ; unit n: rows 8 (n >> 1) .. + 8
    v_lshrrev_b32 v3, 3, v1
    v_and_b32 v4, 7, v1
    s_lshr_b32 s29, s28, 1
    s_lshl_b32 s29, s29, 6
    s_add_u32 s43, s29, s25
    s_mulk_i32 s43, 896
    v_mul_u32_u24 v3, 56, v3
    v_min_u32 v4, 6, v4
    v_add_lshl_u32 v19, v4, v3, 7
    s_xor_b32 s3, s24, s26
    s_cmp_ge_u32 s24, NOW
    s_cbranch_scc1 .Lg_fetch
    s_bitcmp1_b32 s3, 0
    s_waitcnt lgkmcnt(0)
    s_cselect_b64 s[8:9], s[54:55], s[52:53]   ; waves alternate between the two matrices
    buffer_load_dword v5, v19, s[8:11], s43 offen
    s_branch .Lg_fetch
    ; ================ worker, waves 0..7 (loaders, later finishers): x image, then 8 steps of 16 weight rows each ================
.Lg_work:
    s_cmp_gt_u32 s24, 7
    s_cbranch_scc1 .Lg_wextra0
    s_and_b32 s28, s26, 1
    s_lshl1_add_u32 s28, s24, s28        ; gate and up workers start on different halves of x
    s_lshl_b32 s29, s28, 12
    s_lshl_b32 s28, s28, 1
    s_and_b32 s28, s28, 14
    s_mov_b32 m0, s29
    v_xor_b32 v2, s28, v1
    s_lshl_b32 s6, 1, 16
    s_mov_b32 s7, s11
    v_lshl_add_u32 v2, v2, 4, s29
    s_setprio 3
    s_waitcnt lgkmcnt(0)
    buffer_load_dwordx4 v2, s[4:7], 0 offen lds
    buffer_load_dwordx4 v2, s[4:7], 0 offen offset:1024 lds
    v_xor_b32 v2, 16, v2
    buffer_load_dwordx4 v2, s[4:7], 0 offen offset:2048 lds
    buffer_load_dwordx4 v2, s[4:7], 0 offen offset:3072 lds
    s_xor_b32 m0, m0, 0x1000
    v_xor_b32 v2, 0x1030, v2
    buffer_load_dwordx4 v2, s[4:7], 0 offen lds
    buffer_load_dwordx4 v2, s[4:7], 0 offen offset:1024 lds
    v_xor_b32 v2, 16, v2
    buffer_load_dwordx4 v2, s[4:7], 0 offen offset:2048 lds
    buffer_load_dwordx4 v2, s[4:7], 0 offen offset:3072 lds
    s_cmp_gt_u32 s24, 7
.Lg_wextra0:                             ; waves 8..15 pass through here: their fetch brings the code below in early
    s_cbranch_scc1 .Lg_wextra
    s_setprio 2
    s_bitcmp1_b32 s2, 3
    s_cselect_b64 s[8:9], s[54:55], s[52:53]
    v_lshrrev_b32 v4, 2, v1
    v_lshrrev_b32 v3, 3, v1
    v_and_b32 v4, 4, v4
    v_mul_u32_u24 v3, 7168, v3
    v_xor_b32 v4, v4, v1
    s_mul_i32 s30, s25, 7
    s_lshr_b32 s31, s2, 4
    v_and_b32 v4, 7, v4
    s_add_u32 s30, s30, s31              ; tile
    s_mul_i32 s31, s24, 114688           ; 16 rows
    s_load_dwordx2 s[16:17], s[0:1], 40
    v_lshl_add_u32 v3, v4, 4, v3         ; per-lane source offset
    s_lshl_b32 s33, s30, 7
    s_mul_i32 s32, s24, L_REG
    s_add_u32 s31, s31, s33              ; running source offset
    s_add_u32 s32, s32, L_W              ; this wave's LDS region
    .macro WSTEP slot
    s_add_u32 m0, s32, \slot*2048
    s_add_u32 s33, s31, 57344
    buffer_load_dwordx4 v3, s[8:11], s31 offen lds
    s_add_u32 m0, s32, \slot*2048+1024
    s_add_u32 s31, s31, 917504
    buffer_load_dwordx4 v3, s[8:11], s33 offen lds
    .endm
    ; this wave finishes the 16 x 16 tile (w >> 2, w & 3): x fragments of the last four steps v8, v9, v12, v13
    v_and_b32 v5, 15, v1
    v_lshrrev_b32 v6, 4, v1
    v_xor_b32 v7, v6, v5
    v_lshlrev_b32 v7, 4, v7
    v_lshl_add_u32 v7, v5, 11, v7
    v_lshrrev_b32 v4, 5, v1
    s_lshr_b32 s28, s24, 2
    s_lshl_b32 s29, s28, 15
    v_add_u32 v8, s29, v7
    v_xor_b32 v9, 64, v8
    v_xor_b32 v12, 0x80, v8
    v_xor_b32 v13, 0xc0, v8
    ; v16 / v17 transposed weight reads, v18 partial sums, v20 slot offset, v22..v25 gelu constants
    s_and_b32 s29, s24, 3
    v_bfe_u32 v7, v1, 2, 2               ; trq
    v_and_b32 v10, 2, v7
    v_and_b32 v11, 1, v6
    v_xor_b32 v10, v10, v11
    v_xor_b32 v10, s29, v10
    v_lshlrev_b32 v10, 5, v10
    v_lshl_add_u32 v10, v7, 7, v10
    v_lshl_add_u32 v10, v11, 10, v10
    v_lshl_add_u32 v10, v4, 12, v10
    v_and_b32 v7, 3, v1
    v_lshl_add_u32 v16, v7, 3, v10
    v_add_u32 v17, L_F, v16
    s_lshr_b32 s37, s29, 1
    s_mul_i32 s38, s37, L_REG
    s_and_b32 s39, s24, 1
    s_lshl_b32 s40, s39, 11
    s_add_u32 s38, s38, s40
    s_lshl_b32 s40, s28, 8
    s_add_u32 s38, s38, s40
    s_add_u32 s38, s38, L_W
    v_lshlrev_b32 v5, 4, v5
    v_lshl_add_u32 v10, v6, 9, v5
    v_add_u32 v18, s38, v10
    s_mov_b32 s18, 0x300000
    s_mov_b32 s19, s11
    s_bfe_u32 s36, s2, 0x10003
    s_mul_i32 s36, s36, H_MAT
    s_lshl_b32 s38, s30, 12
    s_add_u32 s36, s36, s38
    s_lshl_b32 s37, s37, 11
    s_lshl_b32 s38, s28, 10
    s_lshl_b32 s39, s39, 9
    s_add_u32 s36, s36, s37
    s_add_u32 s36, s36, s38
    s_add_u32 s36, s36, s39
    s_add_u32 s36, s36, WS_H
    v_lshl_add_u32 v5, v4, 8, v5
    v_mov_b32 v21, OOB
    v_cmp_eq_u32 vcc, 0, v11
    v_mov_b32 v22, 0xbdd2d3e8            ; -2 sqrt(2/pi) log2(e) 0.044715
    v_mov_b32 v24, 0xc0135761            ; -2 sqrt(2/pi) log2(e)
    v_cndmask_b32 v20, v21, v5, vcc
    v_mov_b32 v23, v22
    v_mov_b32 v25, v24
    s_mov_b32 s37, 0xffff0000
    v_xor_b32 v2, 32, v3
    WSTEP 0
    s_setprio 1
    WSTEP 1
    s_waitcnt vmcnt(4)
    s_barrier                            ; x is in LDS
    s_setprio 3
    WSTEP 2
    ds_read_b128 v[32:35], v8 offset:1024
    ds_read_b128 v[36:39], v9 offset:1024
    ds_read_b128 v[40:43], v12 offset:1024
    ds_read_b128 v[44:47], v13 offset:1024
    ds_read_b128 v[48:51], v8 offset:1280
    ds_read_b128 v[52:55], v9 offset:1280
    ds_read_b128 v[56:59], v12 offset:1280
    ds_read_b128 v[60:63], v13 offset:1280
    .macro WLAST o
    s_add_u32 m0, s32, \o
    s_add_u32 s33, s31, 57344
    buffer_load_dwordx4 v3, s[8:11], s31 offen lds
    s_add_u32 m0, s32, \o+1024
    s_add_u32 s31, s31, 917504
    buffer_load_dwordx4 v2, s[8:11], s33 offen lds
    .endm
    s_setprio 2
    WSTEP 3
    s_waitcnt lgkmcnt(4)
    ds_read_b128 v[64:67], v8 offset:1536
    ds_read_b128 v[68:71], v9 offset:1536
    ds_read_b128 v[72:75], v12 offset:1536
    ds_read_b128 v[76:79], v13 offset:1536
    ds_read_b128 v[80:83], v8 offset:1792
    ds_read_b128 v[84:87], v9 offset:1792
    ds_read_b128 v[88:91], v12 offset:1792
    ds_read_b128 v[92:95], v13 offset:1792
    s_lshl_b32 s32, s24, 12
    s_setprio 1
    WLAST L_F
    s_waitcnt vmcnt(6) lgkmcnt(0)
    s_barrier                            ; steps 0, 1 are in LDS, x is in registers
    s_setprio 2
    WLAST L_F+2048
    s_setprio 1
    WLAST 0
    s_waitcnt vmcnt(6)
    s_barrier                            ; steps 2, 3
    WLAST 2048
    s_waitcnt vmcnt(6)
    s_barrier                            ; step 4 and the four partial sums of steps 0..3
    ds_read_b128 v[28:31], v18
    ds_read_b128 v[12:15], v18 offset:2*L_REG
    ds_read_b128 v[112:115], v18 offset:4*L_REG
    ds_read_b128 v[116:119], v18 offset:6*L_REG
    ds_read_b64_tr_b16 v[96:97], v17 offset:0
    ds_read_b64_tr_b16 v[98:99], v17 offset:512
    ds_read_b64_tr_b16 v[100:101], v17 offset:8192
    ds_read_b64_tr_b16 v[102:103], v17 offset:8704
    ds_read_b64_tr_b16 v[104:105], v17 offset:16384
    ds_read_b64_tr_b16 v[106:107], v17 offset:16896
    ds_read_b64_tr_b16 v[108:109], v17 offset:24576
    ds_read_b64_tr_b16 v[110:111], v17 offset:25088
    s_waitcnt lgkmcnt(8)
    v_pk_add_f32 v[28:29], v[28:29], v[12:13]
    v_pk_add_f32 v[30:31], v[30:31], v[14:15]
    v_pk_add_f32 v[112:113], v[112:113], v[116:117]
    v_pk_add_f32 v[114:115], v[114:115], v[118:119]
    v_pk_add_f32 v[28:29], v[28:29], v[112:113]
    v_pk_add_f32 v[30:31], v[30:31], v[114:115]
    s_waitcnt vmcnt(4) lgkmcnt(6)
    s_barrier                            ; step 5
    ds_read_b64_tr_b16 v[112:113], v17 offset:2048
    ds_read_b64_tr_b16 v[114:115], v17 offset:2560
    ds_read_b64_tr_b16 v[116:117], v17 offset:10240
    ds_read_b64_tr_b16 v[118:119], v17 offset:10752
    ds_read_b64_tr_b16 v[120:121], v17 offset:18432
    ds_read_b64_tr_b16 v[122:123], v17 offset:18944
    ds_read_b64_tr_b16 v[124:125], v17 offset:26624
    ds_read_b64_tr_b16 v[126:127], v17 offset:27136
    s_waitcnt lgkmcnt(8)
    v_mfma_f32_16x16x32_bf16 v[28:31], v[96:99], v[32:35], v[28:31]
    v_mfma_f32_16x16x32_bf16 v[28:31], v[100:103], v[36:39], v[28:31]
    v_mfma_f32_16x16x32_bf16 v[28:31], v[104:107], v[40:43], v[28:31]
    v_mfma_f32_16x16x32_bf16 v[28:31], v[108:111], v[44:47], v[28:31]
    s_waitcnt vmcnt(2) lgkmcnt(6)
    s_barrier                            ; step 6
    ds_read_b64_tr_b16 v[96:97], v16 offset:0
    ds_read_b64_tr_b16 v[98:99], v16 offset:512
    ds_read_b64_tr_b16 v[100:101], v16 offset:8192
    ds_read_b64_tr_b16 v[102:103], v16 offset:8704
    ds_read_b64_tr_b16 v[104:105], v16 offset:16384
    ds_read_b64_tr_b16 v[106:107], v16 offset:16896
    )ASM" R"ASM(
    ds_read_b64_tr_b16 v[108:109], v16 offset:24576
    ds_read_b64_tr_b16 v[110:111], v16 offset:25088
    s_waitcnt lgkmcnt(8)
    v_mfma_f32_16x16x32_bf16 v[28:31], v[112:115], v[48:51], v[28:31]
    v_mfma_f32_16x16x32_bf16 v[28:31], v[116:119], v[52:55], v[28:31]
    v_mfma_f32_16x16x32_bf16 v[28:31], v[120:123], v[56:59], v[28:31]
    v_mfma_f32_16x16x32_bf16 v[28:31], v[124:127], v[60:63], v[28:31]
    s_waitcnt vmcnt(0) lgkmcnt(6)
    s_barrier                            ; step 7
    ds_read_b64_tr_b16 v[112:113], v16 offset:2048
    ds_read_b64_tr_b16 v[114:115], v16 offset:2560
    ds_read_b64_tr_b16 v[116:117], v16 offset:10240
    ds_read_b64_tr_b16 v[118:119], v16 offset:10752
    ds_read_b64_tr_b16 v[120:121], v16 offset:18432
    ds_read_b64_tr_b16 v[122:123], v16 offset:18944
    ds_read_b64_tr_b16 v[124:125], v16 offset:26624
    ds_read_b64_tr_b16 v[126:127], v16 offset:27136
    s_waitcnt lgkmcnt(8)
    v_mfma_f32_16x16x32_bf16 v[28:31], v[96:99], v[64:67], v[28:31]
    v_mfma_f32_16x16x32_bf16 v[28:31], v[100:103], v[68:71], v[28:31]
    v_mfma_f32_16x16x32_bf16 v[28:31], v[104:107], v[72:75], v[28:31]
    v_mfma_f32_16x16x32_bf16 v[28:31], v[108:111], v[76:79], v[28:31]
    s_waitcnt lgkmcnt(6)
    v_mfma_f32_16x16x32_bf16 v[28:31], v[112:115], v[80:83], v[28:31]
    s_waitcnt lgkmcnt(4)
    v_mfma_f32_16x16x32_bf16 v[28:31], v[116:119], v[84:87], v[28:31]
    s_waitcnt lgkmcnt(2)
    v_mfma_f32_16x16x32_bf16 v[28:31], v[120:123], v[88:91], v[28:31]
    s_waitcnt lgkmcnt(0)
    v_mfma_f32_16x16x32_bf16 v[28:31], v[124:127], v[92:95], v[28:31]
    s_bitcmp1_b32 s2, 3
    s_nop 6
    v_cvt_pk_bf16_f32 v36, v28, v29
    v_cvt_pk_bf16_f32 v37, v30, v31
    s_cbranch_scc1 .Lg_wpub
    ; gelu(g) = g / (1 + exp2(-g (k0 + k1 g^2)))
    v_lshlrev_b32 v32, 16, v36
    v_and_b32 v33, s37, v36
    v_lshlrev_b32 v34, 16, v37
    v_and_b32 v35, s37, v37
    v_pk_mul_f32 v[40:41], v[32:33], v[32:33]
    v_pk_mul_f32 v[42:43], v[34:35], v[34:35]
    v_pk_fma_f32 v[40:41], v[40:41], v[22:23], v[24:25]
    v_pk_fma_f32 v[42:43], v[42:43], v[22:23], v[24:25]
    v_pk_mul_f32 v[40:41], v[40:41], v[32:33]
    v_pk_mul_f32 v[42:43], v[42:43], v[34:35]
    v_exp_f32 v40, v40
    v_exp_f32 v41, v41
    v_exp_f32 v42, v42
    v_exp_f32 v43, v43
    v_pk_add_f32 v[40:41], v[40:41], 1.0 op_sel_hi:[1,0]
    v_pk_add_f32 v[42:43], v[42:43], 1.0 op_sel_hi:[1,0]
    v_rcp_f32 v40, v40
    v_rcp_f32 v41, v41
    v_rcp_f32 v42, v42
    v_rcp_f32 v43, v43
    v_pk_mul_f32 v[40:41], v[40:41], v[32:33]
    v_pk_mul_f32 v[42:43], v[42:43], v[34:35]
    v_cvt_pk_bf16_f32 v36, v40, v41
    v_cvt_pk_bf16_f32 v37, v42, v43
.Lg_wpub:
    v_min_u32 v36, -2, v36               ; keep the "not written yet" pattern out of the data (both are NaN pairs)
    v_min_u32 v37, -2, v37
    v_mov_b32 v40, v36
    v_mov_b32 v41, v37
    s_nop 0
    v_permlane16_swap_b32 v40, v38
    v_permlane16_swap_b32 v41, v39
    buffer_store_dwordx4 v[36:39], v20, s[16:19], s36 offen sc1
    s_endpgm

    ; ================ worker, waves 8..15: 32 x 32 partial sums over the rows of two loaders, steps 0..3 ================
.Lg_wextra:
    s_load_dwordx2 s[16:17], s[0:1], 40
    s_mul_i32 s30, s25, 7
    s_lshr_b32 s31, s2, 4
    s_add_u32 s30, s30, s31
    s_cmp_lg_u32 s24, 8
    s_cbranch_scc1 .Lg_wx0
    ; wave 8: mark the slot "not written yet", read the epoch
    s_setprio 3
    s_mov_b32 s18, 0x300000
    s_mov_b32 s19, s11
    s_bfe_u32 s36, s2, 0x10003
    s_mul_i32 s36, s36, H_MAT
    s_lshl_b32 s37, s30, 12
    s_add_u32 s36, s36, s37
    s_add_u32 s36, s36, WS_H
    v_mov_b32 v28, -1
    v_mov_b32 v29, -1
    v_mov_b32 v30, -1
    v_mov_b32 v31, -1
    v_lshlrev_b32 v4, 4, v1
    s_waitcnt lgkmcnt(0)
    buffer_store_dwordx4 v[28:31], v4, s[16:19], s36 offen sc1
    buffer_store_dwordx4 v[28:31], v4, s[16:19], s36 offen offset:1024 sc1
    buffer_store_dwordx4 v[28:31], v4, s[16:19], s36 offen offset:2048 sc1
    buffer_store_dwordx4 v[28:31], v4, s[16:19], s36 offen offset:3072 sc1
    s_mov_b64 exec, 1
    buffer_load_dwordx2 v[26:27], off, s[16:19], 0 sc1
    s_mov_b64 exec, -1
.Lg_wx0:
    ; LDS addresses: x fragments v8 / v9, transposed weight reads v16, partial sums v18
    s_sub_u32 s28, s24, 8
    s_lshr_b32 s29, s28, 1               ; K range
    s_and_b32 s31, s28, 1                ; column half
    v_lshrrev_b32 v5, 5, v1
    v_and_b32 v6, 31, v1
    v_xor_b32 v7, v5, v1
    s_lshl_b32 s32, s29, 2
    v_xor_b32 v7, s32, v7
    v_lshlrev_b32 v4, 11, v6
    v_and_b32 v7, 15, v7
    v_lshl_add_u32 v8, v7, 4, v4
    v_xor_b32 v9, 32, v8
    s_barrier                            ; x is in LDS
    ds_read_b128 v[32:35], v8 offset:0
    ds_read_b128 v[64:67], v9 offset:0
    ds_read_b128 v[36:39], v8 offset:256
    ds_read_b128 v[68:71], v9 offset:256
    ds_read_b128 v[40:43], v8 offset:512
    ds_read_b128 v[72:75], v9 offset:512
    ds_read_b128 v[44:47], v8 offset:768
    ds_read_b128 v[76:79], v9 offset:768
    v_lshlrev_b32 v6, 4, v1
    s_mul_i32 s33, s28, L_REG
    s_add_u32 s33, s33, L_W
    v_add_u32 v18, s33, v6
    v_lshlrev_b32 v6, 10, v5             ; (lane >> 5) * 1024
    v_bfe_u32 v7, v1, 2, 2               ; trq
    v_lshl_add_u32 v6, v7, 7, v6
    v_and_b32 v7, 16, v1
    v_lshl_add_u32 v6, v7, 1, v6
    v_and_b32 v7, 3, v1
    v_lshl_add_u32 v6, v7, 3, v6
    v_and_b32 v7, 8, v1
    v_lshl_add_u32 v6, v7, 3, v6
    s_lshl_b32 s33, s31, 6
    v_xor_b32 v6, s33, v6
    s_mul_i32 s33, s29, 2*L_REG
    s_add_u32 s33, s33, L_W
    v_add_u32 v16, s33, v6
    .macro RD a, o, d, r
    ds_read_b64_tr_b16 v[\r:\r+1], \a offset:\o
    ds_read_b64_tr_b16 v[\r+2:\r+3], \a offset:\o+512
    ds_read_b64_tr_b16 v[\r+4:\r+5], \a offset:\o+\d
    ds_read_b64_tr_b16 v[\r+6:\r+7], \a offset:\o+\d+512
    .endm
    ; v14 / s41: a few w_down lines this wave pulls into L2 once the workgroup's own loads are all issued
    s_load_dwordx2 s[12:13], s[0:1], 24
    s_mov_b32 s14, s10
    s_mov_b32 s15, s11
    s_and_b32 s41, s2, -8
    s_add_u32 s41, s41, s28              ; li * 8 + j
    s_and_b32 s42, s41, 3
    s_lshr_b32 s41, s41, 2
    s_xor_b32 s43, s25, 4
    s_and_b32 s43, s43, 6
    s_lshl_b32 s43, s43, 1
    s_add_u32 s42, s42, s43
    s_mul_i32 s42, s42, 224
    s_add_u32 s42, s42, 224-SPW*4-WT
    s_mul_i32 s41, s41, WT
    s_sub_u32 s41, s42, s41
    s_lshl_b32 s41, s41, 1
    s_and_b32 s42, s25, 1
    s_add_u32 s41, s41, s42
    s_lshl_b32 s41, s41, 10
    v_min_u32 v14, WT*8-1, v1
    v_lshrrev_b32 v15, 3, v14
    v_and_b32 v14, 7, v14
    v_lshlrev_b32 v14, 7, v14
    v_lshl_add_u32 v14, v15, 11, v14
    s_waitcnt lgkmcnt(0)
    s_barrier                            ; steps 0, 1 are in LDS, x is in registers
    RD v16, 0, L_REG, 112
    RD v16, 2048, L_REG, 120
    s_waitcnt lgkmcnt(6)
    v_mfma_f32_32x32x16_bf16 v[96:111], v[112:115], v[32:35], 0
    s_waitcnt lgkmcnt(4)
    v_mfma_f32_32x32x16_bf16 v[96:111], v[116:119], v[64:67], v[96:111]
    s_waitcnt lgkmcnt(2)
    v_mfma_f32_32x32x16_bf16 v[96:111], v[120:123], v[36:39], v[96:111]
    s_waitcnt lgkmcnt(0)
    v_mfma_f32_32x32x16_bf16 v[96:111], v[124:127], v[68:71], v[96:111]
    ; wave 8: its marking stores are complete -> publish the flag (this call's tag = epoch + 1)
    s_cmp_lg_u32 s24, 8
    s_cbranch_scc1 .Lg_wx1
    s_lshl_b32 s44, s25, 5
    s_lshr_b32 s45, s2, 3
    s_add_u32 s44, s44, s45
    s_lshl_b32 s45, s44, 7
    s_add_u32 s45, s45, WS_FLAG
    s_mov_b64 exec, 1
    s_waitcnt vmcnt(0)
    v_add_co_u32 v26, vcc, 1, v26
    s_nop 1
    v_addc_co_u32 v27, vcc, 0, v27, vcc
    buffer_store_dwordx2 v[26:27], off, s[16:19], s45 sc1
    s_mov_b64 exec, -1
    s_setprio 0
.Lg_wx1:
    s_barrier                            ; steps 2, 3
    RD v16, 4096, L_REG, 112
    RD v16, 6144, L_REG, 120
    s_waitcnt lgkmcnt(6)
    v_mfma_f32_32x32x16_bf16 v[96:111], v[112:115], v[40:43], v[96:111]
    s_waitcnt lgkmcnt(4)
    v_mfma_f32_32x32x16_bf16 v[96:111], v[116:119], v[72:75], v[96:111]
    s_waitcnt lgkmcnt(2)
    v_mfma_f32_32x32x16_bf16 v[96:111], v[120:123], v[44:47], v[96:111]
    s_waitcnt lgkmcnt(0)
    v_mfma_f32_32x32x16_bf16 v[96:111], v[124:127], v[76:79], v[96:111]
    s_nop 11
    ds_write_b128 v18, v[96:99]
    ds_write_b128 v18, v[100:103] offset:RED
    ds_write_b128 v18, v[104:107] offset:2*RED
    ds_write_b128 v18, v[108:111] offset:3*RED
    s_waitcnt lgkmcnt(0)
    s_barrier                            ; step 4 and the partial sums
    buffer_load_dword v15, v14, s[12:15], s41 offen
    s_barrier                            ; step 5
    s_barrier                            ; step 6
    s_barrier                            ; step 7
    s_endpgm

    ; ================ the other 18 workgroups of a group: first pull its gate/up lines into L2, one unit per wave, paced ================
.Lg_fetch:
    s_load_dwordx2 s[16:17], s[0:1], 40
    s_mov_b32 s18, 0x300000
    s_mov_b32 s19, s11
    s_cmp_lg_u32 s24, 15
    s_cbranch_scc1 .Lg_fe
    ; wave 15 has no unit: it fetches the epoch for the workgroup
    )ASM" R"ASM(
    s_setprio 3
    s_mov_b64 exec, 1
    s_waitcnt lgkmcnt(0)
    buffer_load_dwordx2 v[26:27], off, s[16:19], 0 sc1
    v_mov_b32 v5, L_TAG
    s_lshl_b32 s30, s25, 5
    s_add_u32 s30, s30, s26
    s_lshl_b32 s30, s30, 7
    s_add_u32 s30, s30, WS_FLAG
    s_waitcnt vmcnt(0)
    v_add_co_u32 v26, vcc, 1, v26
    s_nop 1
    v_addc_co_u32 v27, vcc, 0, v27, vcc
    ds_write_b32 v5, v26
    buffer_store_dwordx2 v[26:27], off, s[16:19], s30 sc1
    s_mov_b64 exec, -1
    s_waitcnt lgkmcnt(0)
    s_branch .Lg_f2
.Lg_fe:
    s_cmp_lg_u32 s28, CPFN
    s_cbranch_scc1 .Lg_fe1
    s_getpc_b64 s[30:31]                 ; one wave per group pulls this kernel's code into L2
.Lg_pc:
    s_sub_u32 s30, s30, .Lg_pc-.Lg_start
    s_subb_u32 s31, s31, 0
    v_lshlrev_b32 v6, 7, v1
    v_min_u32 v6, .Lg_end-.Lg_start, v6
    global_load_dword v60, v6, s[30:31]
.Lg_fe1:
    s_bitcmp1_b32 s3, 0
    s_waitcnt lgkmcnt(0)
    s_cselect_b64 s[8:9], s[54:55], s[52:53]
    s_cmp_lt_u32 s24, NOW
    s_cbranch_scc1 .Lg_f2
    s_cmp_ge_u32 s24, FIRST
    s_cbranch_scc1 .Lg_f2
    .macro PACEW tag
    s_bitcmp1_b32 s47, 0
    s_cbranch_scc0 .Lg_pa\tag
    s_sleep (PQ+2)/4
.Lg_pa\tag:
    s_bitcmp1_b32 s47, 1
    s_cbranch_scc0 .Lg_pb\tag
    s_sleep (2*PQ+2)/4
.Lg_pb\tag:
    s_bitcmp1_b32 s47, 2
    s_cbranch_scc0 .Lg_pc\tag
    s_sleep PQ
.Lg_pc\tag:
    .endm
    s_sub_u32 s47, s24, PB
    PACEW 0
    buffer_load_dword v5, v19, s[8:11], s43 offen
.Lg_f2:
    s_load_dwordx2 s[12:13], s[0:1], 24
    s_load_dwordx2 s[20:21], s[0:1], 32
    s_mov_b32 s14, s10
    s_mov_b32 s15, s11
    s_xor_b32 s31, s25, 4
    s_and_b32 s31, s31, 6
    s_lshl_b32 s31, s31, 1               ; first of the four K slices this group consumes (produced by groups on other XCDs)
    s_and_b32 s28, s27, 3
    s_add_u32 s31, s31, s28              ; s: K slice
    s_and_b32 s32, s25, 1
    s_lshl_b32 s32, s32, 2
    s_lshr_b32 s28, s27, 2
    s_add_u32 s32, s32, s28              ; cg
    v_lshrrev_b32 v6, 4, v1              ; kg
    v_and_b32 v7, 15, v1
    s_cmp_gt_u32 s26, 29
    s_cbranch_scc1 .Lg_spare
    ; ================ down-projection workgroup: K slice s (224 hidden units) x 128 output columns ================
    ; v8 / v9: w_down source, s33: its uniform part
    v_lshlrev_b32 v8, 1, v6
    v_xor_b32 v8, v8, v7
    v_lshlrev_b32 v8, 4, v8
    v_lshl_add_u32 v8, v6, 11, v8
    v_xor_b32 v9, 128, v8
    s_mul_i32 s33, s31, 224
    s_lshl_b32 s29, s24, 4
    s_add_u32 s33, s33, s29
    s_lshl_b32 s33, s33, 11
    s_lshl_b32 s29, s32, 8
    s_add_u32 s33, s33, s29
    ; v10: data of the two feeding workers
    s_mul_i32 s29, s31, 14
    s_add_u32 s29, s29, s24
    s_lshl_b32 s29, s29, 10
    s_add_u32 s29, s29, WS_H
    v_lshlrev_b32 v4, 4, v1
    v_add_u32 v10, s29, v4
    ; v11: transposed reads of the w_down image
    v_bfe_u32 v3, v1, 2, 2               ; trq
    v_and_b32 v11, 16, v1
    v_lshrrev_b32 v11, 2, v11
    v_or_b32 v11, v11, v3
    s_and_b32 s29, s24, 7
    v_xor_b32 v11, s29, v11
    v_lshlrev_b32 v11, 5, v11
    v_and_b32 v2, 3, v1
    v_lshl_add_u32 v11, v2, 3, v11
    v_lshl_add_u32 v11, v3, 8, v11
    v_lshl_add_u32 v11, v6, 11, v11
    ; v12: h write, v13: h read
    s_lshl_b32 s35, s24, 10
    s_add_u32 s29, s35, L_H
    v_add_u32 v12, s29, v4
    s_lshr_b32 s29, s24, 3
    s_lshl_b32 s29, s29, 10
    s_add_u32 s29, s29, L_H
    v_add_u32 v13, s29, v4
    ; v14: partial out, v15: partials in
    s_lshl_b32 s29, s32, 4
    s_add_u32 s30, s29, s24
    s_lshl_b32 s30, s30, 4
    s_add_u32 s30, s30, s31
    s_lshl_b32 s30, s30, 10
    s_add_u32 s30, s30, WS_PC
    v_add_u32 v14, s30, v4
    s_xor_b32 s32, s32, 4                ; reduces a tile of the other column half: its 16 partials all come from other XCDs
    s_lshl_b32 s29, s32, 4
    s_add_u32 s29, s29, s31
    s_lshl_b32 s29, s29, 14
    s_lshl_b32 s30, s24, 7
    s_add_u32 s29, s29, s30
    s_add_u32 s29, s29, WS_PC
    v_and_b32 v15, 56, v1
    v_and_b32 v2, 7, v1
    v_lshlrev_b32 v15, 7, v15
    v_lshl_add_u32 v15, v2, 4, v15
    v_add_u32 v15, s29, v15
    ; v16: output
    s_lshr_b32 s29, s31, 3
    s_lshl_b32 s29, s29, 1
    s_and_b32 s30, s24, 1
    s_add_u32 s29, s29, s30
    s_lshl_b32 s29, s29, 14
    s_lshl_b32 s30, s32, 8
    s_add_u32 s29, s29, s30
    s_and_b32 s30, s31, 7
    s_lshl_b32 s30, s30, 5
    s_add_u32 s29, s29, s30
    s_lshr_b32 s30, s24, 1
    s_lshl_b32 s30, s30, 3
    s_add_u32 s29, s29, s30
    v_lshlrev_b32 v16, 11, v2
    v_lshl_add_u32 v16, v6, 2, v16
    v_add_u32 v16, s29, v16
    v_and_b32 v3, 40, v1
    v_mov_b32 v4, OOB
    v_cmp_eq_u32 vcc, 0, v3
    s_mov_b32 s22, 0x10000
    s_mov_b32 s23, s11
    v_cndmask_b32 v16, v4, v16, vcc
    s_mov_b32 s36, H_MAT
    s_mov_b32 s37, 0xffff0000
    s_movk_i32 s38, 0xff
    s_mov_b32 s39, 0xffffff00
    s_mov_b32 s40, 0x0c0c0400
    s_mov_b32 s41, 0x05040100
    s_mov_b32 s46, 0x01010101
    s_lshl_b32 m0, s24, 12
    s_add_u32 s29, s33, 7168
    s_add_u32 s30, s33, 2*7168
    s_add_u32 s34, s33, 3*7168
    v_mov_b32 v4, L_TAG
    s_mov_b32 s42, 0x10000
    s_sub_u32 s47, s24, FIRST
    s_cmp_gt_u32 s47, 14-FIRST           ; waves below FIRST have issued theirs, wave 15 has none
    s_cbranch_scc1 .Lg_f4
    s_cmp_ge_u32 s43, s10
    s_cbranch_scc1 .Lg_f4
    s_sleep (8-PB)*PQ/4-CQ
    PACEW 1
    buffer_load_dword v5, v19, s[8:11], s43 offen
.Lg_f4:
    s_cmp_gt_u32 s24, 13
    s_waitcnt lgkmcnt(0)
    s_barrier                            ; every prefetch of this workgroup is issued
    s_cbranch_scc1 .Lg_pserv
    s_sleep WDS
    buffer_load_dwordx4 v8, s[12:15], s33 offen lds
    buffer_load_dwordx4 v8, s[12:15], s29 offen offset:1024 lds
    buffer_load_dwordx4 v9, s[12:15], s30 offen offset:2048 lds
    buffer_load_dwordx4 v9, s[12:15], s34 offen offset:3072 lds
.Lg_p1:
    ds_read_b32 v26, v4
    s_waitcnt vmcnt(0) lgkmcnt(0)
    s_barrier                            ; the w_down tile is in LDS; the feeding slots were marked in this call
    s_cbranch_scc1 .Lg_p2
    buffer_load_dwordx4 v[64:67], v10, s[16:19], 0 offen sc1
    buffer_load_dwordx4 v[68:71], v10, s[16:19], s36 offen sc1
.Lg_p2:
    ds_read_b64_tr_b16 v[32:33], v11
    ds_read_b64_tr_b16 v[34:35], v11 offset:1024
    ds_read_b64_tr_b16 v[36:37], v11 offset:8192
    ds_read_b64_tr_b16 v[38:39], v11 offset:9216
    ds_read_b64_tr_b16 v[40:41], v11 offset:16384
    ds_read_b64_tr_b16 v[42:43], v11 offset:17408
    ds_read_b64_tr_b16 v[44:45], v11 offset:24576
    ds_read_b64_tr_b16 v[46:47], v11 offset:25600
    ds_read_b64_tr_b16 v[48:49], v11 offset:32768
    ds_read_b64_tr_b16 v[50:51], v11 offset:33792
    ds_read_b64_tr_b16 v[52:53], v11 offset:40960
    ds_read_b64_tr_b16 v[54:55], v11 offset:41984
    ds_read_b64_tr_b16 v[56:57], v11 offset:49152
    ds_read_b64_tr_b16 v[58:59], v11 offset:50176
    v_mul_lo_u32 v31, v26, s46           ; four tag bytes: one-to-one in the tag, every byte differs from the previous call's
    v_lshrrev_b32 v27, 8, v31
    v_lshrrev_b32 v28, 16, v31
    v_lshrrev_b32 v29, 24, v31
    s_cbranch_scc1 .Lg_p6
.Lg_p4:                                  ; every dword of gelu(g) and u has replaced the mark
    s_waitcnt vmcnt(0)
    v_max3_u32 v2, v64, v65, v66
    v_max3_u32 v3, v67, v68, v69
    v_max3_u32 v2, v2, v70, v71
    v_max_u32 v2, v2, v3
    v_cmp_eq_u32 vcc, -1, v2
    s_cbranch_vccnz .Lg_pretry
.Lg_p5:                                  ; h = bf16(gelu(g) * u)
    v_lshlrev_b32 v72, 16, v64
    v_and_b32 v73, s37, v64
    v_lshlrev_b32 v74, 16, v68
    v_and_b32 v75, s37, v68
    v_lshlrev_b32 v76, 16, v65
    v_and_b32 v77, s37, v65
    v_lshlrev_b32 v78, 16, v69
    v_and_b32 v79, s37, v69
    v_pk_mul_f32 v[72:73], v[72:73], v[74:75]
    v_lshlrev_b32 v80, 16, v66
    v_and_b32 v81, s37, v66
    v_pk_mul_f32 v[76:77], v[76:77], v[78:79]
    v_lshlrev_b32 v82, 16, v70
    v_and_b32 v83, s37, v70
    v_cvt_pk_bf16_f32 v64, v72, v73
    v_lshlrev_b32 v84, 16, v67
    v_and_b32 v85, s37, v67
    v_pk_mul_f32 v[80:81], v[80:81], v[82:83]
    v_lshlrev_b32 v86, 16, v71
    v_and_b32 v87, s37, v71
    v_cvt_pk_bf16_f32 v65, v76, v77
    v_pk_mul_f32 v[84:85], v[84:85], v[86:87]
    v_cvt_pk_bf16_f32 v66, v80, v81
    v_cvt_pk_bf16_f32 v67, v84, v85
    ds_write_b128 v12, v[64:67]
.Lg_p6:
    s_waitcnt lgkmcnt(0)
    s_barrier                            ; h of this K slice is in LDS
    ds_read_b128 v[64:67], v13
    ds_read_b128 v[68:71], v13 offset:2048
    ds_read_b128 v[72:75], v13 offset:4096
    ds_read_b128 v[76:79], v13 offset:6144
    ds_read_b128 v[80:83], v13 offset:8192
    ds_read_b128 v[84:87], v13 offset:10240
    ds_read_b128 v[88:91], v13 offset:12288
    s_waitcnt lgkmcnt(6)
    v_mfma_f32_16x16x32_bf16 v[4:7], v[32:35], v[64:67], 0
    s_waitcnt lgkmcnt(5)
    v_mfma_f32_16x16x32_bf16 v[4:7], v[36:39], v[68:71], v[4:7]
    s_waitcnt lgkmcnt(4)
    v_mfma_f32_16x16x32_bf16 v[4:7], v[40:43], v[72:75], v[4:7]
    s_waitcnt lgkmcnt(3)
    v_mfma_f32_16x16x32_bf16 v[4:7], v[44:47], v[76:79], v[4:7]
    s_waitcnt lgkmcnt(2)
    )ASM" R"ASM(
    v_mfma_f32_16x16x32_bf16 v[4:7], v[48:51], v[80:83], v[4:7]
    s_waitcnt lgkmcnt(1)
    v_mfma_f32_16x16x32_bf16 v[4:7], v[52:55], v[84:87], v[4:7]
    s_waitcnt lgkmcnt(0)
    v_mfma_f32_16x16x32_bf16 v[4:7], v[56:59], v[88:91], v[4:7]
    s_nop 7
    v_bfi_b32 v4, s38, v31, v4           ; low byte of every f32 partial = a tag byte of this call
    v_bfi_b32 v5, s38, v27, v5
    v_bfi_b32 v6, s38, v28, v6
    v_bfi_b32 v7, s38, v29, v7
    buffer_store_dwordx4 v[4:7], v14, s[16:19], 0 offen sc1
    s_cmp_gt_u32 s24, 7
    s_cbranch_scc1 .Lg_end
    s_mov_b32 s42, 0x10000
    s_movk_i32 s29, 0x2000
    s_sleep CDELAY
.Lg_p7:                                    ; lane = (partial, tile lane): all 16 partials carry this call's tag
    buffer_load_dwordx4 v[20:23], v15, s[16:19], 0 offen sc1
    buffer_load_dwordx4 v[8:11], v15, s[16:19], s29 offen sc1
    s_sub_u32 s42, s42, 1
    s_waitcnt vmcnt(1)
    v_perm_b32 v2, v21, v20, s40
    v_perm_b32 v3, v23, v22, s40
    v_and_b32 v20, s39, v20
    v_and_b32 v21, s39, v21
    v_perm_b32 v2, v3, v2, s41
    v_and_b32 v22, s39, v22
    v_and_b32 v23, s39, v23
    v_cmp_ne_u32 s[44:45], v31, v2
    s_waitcnt vmcnt(0)
    v_perm_b32 v2, v9, v8, s40
    v_perm_b32 v3, v11, v10, s40
    v_and_b32 v8, s39, v8
    v_and_b32 v9, s39, v9
    v_perm_b32 v2, v3, v2, s41
    v_and_b32 v10, s39, v10
    v_and_b32 v11, s39, v11
    v_cmp_ne_u32 vcc, v31, v2
    v_pk_add_f32 v[20:21], v[20:21], v[8:9]
    v_pk_add_f32 v[22:23], v[22:23], v[10:11]
    s_cbranch_scc1 .Lg_p8
    s_or_b64 vcc, vcc, s[44:45]
    s_cbranch_scc1 .Lg_p7
.Lg_p8:
    v_permlane32_swap_b32 v20, v21
    v_permlane32_swap_b32 v22, v23
    v_add_f32 v20, v20, v21
    v_add_f32 v21, v22, v23
    s_nop 1
    v_permlane16_swap_b32 v20, v21
    v_add_f32 v20, v20, v21
    s_nop 1
    v_add_f32_dpp v20, v20, v20 row_ror:8 row_mask:0xf bank_mask:0xf
    v_mov_b32 v21, v20
    v_mov_b32 v22, v20
    s_nop 1
    v_permlane32_swap_b32 v21, v22
    v_cvt_pk_bf16_f32 v20, v20, v22
    buffer_store_dword v20, v16, s[20:23], 0 offen
    s_endpgm
.Lg_pretry:
    s_sub_u32 s42, s42, 1
    s_cbranch_scc1 .Lg_p5
    s_sleep HSLEEP
    buffer_load_dwordx4 v[64:67], v10, s[16:19], 0 offen sc1
    buffer_load_dwordx4 v[68:71], v10, s[16:19], s36 offen sc1
    s_branch .Lg_p4
.Lg_pserv:                                 ; wave 15: the eight feeding workers have marked their slots in this call
    s_cmp_eq_u32 s24, 14
    s_cbranch_scc1 .Lg_p1
    v_readfirstlane_b32 s44, v26
    v_readfirstlane_b32 s45, v27
    s_lshr_b32 s29, s31, 1
    s_lshl_b32 s29, s29, 12
    s_and_b32 s30, s31, 1
    s_mul_i32 s30, s30, 768
    s_add_u32 s29, s29, s30
    s_add_u32 s29, s29, WS_FLAG
    v_lshlrev_b32 v2, 7, v1
    s_mov_b64 exec, 0xff
    s_sleep FDELAY
.Lg_ps0:
    buffer_load_dwordx2 v[28:29], v2, s[16:19], s29 offen sc1
    s_sub_u32 s42, s42, 1
    s_waitcnt vmcnt(0)
    v_cmp_ne_u64 vcc, s[44:45], v[28:29]
    s_cbranch_scc1 .Lg_ps1
    s_cbranch_vccnz .Lg_ps0
.Lg_ps1:
    s_mov_b64 exec, -1
    s_setprio 0
    s_cmp_eq_u32 s24, 15
    s_branch .Lg_p1

    ; ================ spare workgroups: pull the last rows of the w_down slices into L2; one wave advances the epoch ================
.Lg_spare:
    s_and_b32 s29, s24, 1
    s_lshl_b32 s29, s29, 1
    s_add_u32 s29, s29, s31
    s_mul_i32 s29, s29, 224
    s_add_u32 s29, s29, 216
    s_lshl_b32 s30, s24, 2
    s_and_b32 s30, s30, -8
    s_sub_u32 s29, s29, s30
    s_lshl_b32 s29, s29, 11
    s_and_b32 s30, s25, 1
    s_lshl_b32 s30, s30, 10
    s_add_u32 s29, s29, s30
    s_cmp_lt_u32 s24, SPW
    s_cselect_b32 s29, s29, OOB
    v_lshrrev_b32 v8, 3, v1
    v_and_b32 v7, 7, v1
    v_lshlrev_b32 v8, 11, v8
    v_lshl_add_u32 v8, v7, 7, v8
    s_sub_u32 s47, s24, FIRST
    s_cmp_gt_u32 s47, 14-FIRST           ; waves below FIRST have issued theirs, wave 15 has none
    s_cbranch_scc1 .Lg_f4s
    s_cmp_ge_u32 s43, s10
    s_cbranch_scc1 .Lg_f4s
    s_sleep (8-PB)*PQ/4-3
    PACEW 2
    buffer_load_dword v5, v19, s[8:11], s43 offen
.Lg_f4s:
    s_waitcnt lgkmcnt(0)
    s_barrier
    buffer_load_dword v9, v8, s[12:15], s29 offen
    s_lshl_b32 s30, s24, 8
    s_or_b32 s30, s30, s2
    s_cmp_lg_u32 s30, 0xfff
    s_cbranch_scc1 .Lg_end
    v_lshlrev_b32 v10, 9, v1
    s_movk_i32 s34, WS_FLAG
    v_readfirstlane_b32 s44, v26
    v_readfirstlane_b32 s45, v27
    s_mov_b32 s42, 0x10000
    s_sleep 40
    v_mov_b32 v26, s44
    v_mov_b32 v27, s45
.Lg_s1:                                    ; every workgroup that uses the epoch has read it once its word carries this call's tag
    buffer_load_dwordx2 v[12:13], v10, s[16:19], s34 offen sc1
    buffer_load_dwordx2 v[14:15], v10, s[16:19], s34 offen offset:128 sc1
    buffer_load_dwordx2 v[16:17], v10, s[16:19], s34 offen offset:256 sc1
    buffer_load_dwordx2 v[18:19], v10, s[16:19], s34 offen offset:384 sc1
    s_waitcnt vmcnt(0)
    v_cmp_ne_u64 s[28:29], v[26:27], v[12:13]
    v_cmp_ne_u64 s[30:31], v[26:27], v[14:15]
    v_cmp_ne_u64 s[32:33], v[26:27], v[16:17]
    v_cmp_ne_u64 vcc, v[26:27], v[18:19]
    s_or_b64 s[28:29], s[28:29], s[30:31]
    s_or_b64 s[32:33], s[32:33], vcc
    s_or_b64 s[28:29], s[28:29], s[32:33]
    s_cbranch_scc0 .Lg_s2
    s_sleep 8
    s_sub_u32 s42, s42, 1
    s_cbranch_scc0 .Lg_s1
.Lg_s2:
    s_mov_b64 exec, 1
    buffer_store_dwordx2 v[26:27], off, s[16:19], 0 sc1
    s_mov_b64 exec, -1
.Lg_end:
    s_endpgm
    )ASM"
    :
    : "{s[0:1]}"(__builtin_amdgcn_kernarg_segment_ptr()), "{s2}"(blockIdx.x), "{v0}"(threadIdx.x)
    : "memory", "vcc", "scc", "v127", "s55");
    __builtin_unreachable();
}

extern "C" void kernel_launch(void* const* d_in, const int* in_sizes, int n_in, void* d_out, int out_size, void* d_ws, size_t ws_size,
                              hipStream_t stream) {
    (void)hipFuncSetAttribute((const void*)geglu_kernel, hipFuncAttributeMaxDynamicSharedMemorySize, LDS_BYTES);
    geglu_kernel<<<256, 1024, LDS_BYTES, stream>>>(d_in[0], d_in[1], d_in[2], d_in[3], d_out, d_ws);
}
```
